```python
import torch

def _softmax(x, axis=-1):
    return torch.softmax(x, dim=axis)
def _gelu(x):
    return torch.nn.functional.gelu(x, approximate="tanh")
def _silu(x):
    return torch.nn.functional.silu(x)
def _sigmoid(x):
    return torch.sigmoid(x)
def _layernorm(x, axis=-1, eps=1e-5):
    m = x.mean(dim=axis, keepdim=True)
    v = x.var(dim=axis, keepdim=True, unbiased=False)
    return (x - m) / torch.sqrt(v + eps)
def _scatter_add(tgt, idx, src, axis=0):
    return tgt.clone().index_add_(axis, idx, src)
def _segment_sum(src, seg, n):
    out = torch.zeros((n,) + src.shape[1:], dtype=src.dtype)
    return out.index_add_(0, seg, src)
def _pad(x, pw):
    flat = tuple(v for pair in reversed(pw) for v in pair)
    return torch.nn.functional.pad(x, flat)

kernel_name = 'gated_mix__mha_block_cached__sliding_block_cached__decode'

def setup_inputs():

    _w = (lambda a: (lambda t: t.to(torch.bfloat16) if t.is_floating_point() else t)(torch.as_tensor(a)))
    def _A_setup():
        B, Sq, D = 4, 1, 512
        H, Sk, Dh = 32, 4096, 128
        return {
            "x": _w((lambda *s: torch.randn(*s, dtype=torch.bfloat16) * 0.05)(B, Sq, D)),
            "k_cache": _w((lambda *s: torch.randn(*s, dtype=torch.bfloat16) * 6.4)(B, H, Sk, Dh)),
            "v_cache": _w((lambda *s: torch.randn(*s, dtype=torch.bfloat16) * 0.05)(B, H, Sk, Dh)),
            "wq": _w((lambda *s: torch.randn(*s, dtype=torch.bfloat16) * 6.4)(D, H * Dh) * (D ** -0.5)),
            "wo": _w((lambda *s: torch.randn(*s, dtype=torch.bfloat16) * 0.05)(H * Dh, D) * ((H * Dh) ** -0.5)),
        }
    _A_in = _A_setup()

    def _B_setup():
        B, Sq, D = 4, 1, 512
        Hq, Hkv, Sk, Dh = 32, 32, 4096, 128
        assert Hq % Hkv == 0, (Hq, Hkv)
        return {
            "x": _w((lambda *s: torch.randn(*s, dtype=torch.bfloat16) * 0.05)(B, Sq, D)),
            "k_cache": _w((lambda *s: torch.randn(*s, dtype=torch.bfloat16) * 6.4)(B, Hkv, Sk, Dh)),
            "v_cache": _w((lambda *s: torch.randn(*s, dtype=torch.bfloat16) * 0.05)(B, Hkv, Sk, Dh)),
            "wq": _w((lambda *s: torch.randn(*s, dtype=torch.bfloat16) * 6.4)(D, Hq * Dh) * (D ** -0.5)),
            "wo": _w((lambda *s: torch.randn(*s, dtype=torch.bfloat16) * 0.05)(Hq * Dh, D) * ((Hq * Dh) ** -0.5)),
        }
    _B_in = _B_setup()

    return {
        "A_x": _A_in["x"],
        "A_k_cache": _A_in["k_cache"],
        "A_v_cache": _A_in["v_cache"],
        "A_wq": _A_in["wq"],
        "A_wo": _A_in["wo"],
        "B_x": _B_in["x"],
        "B_k_cache": _B_in["k_cache"],
        "B_v_cache": _B_in["v_cache"],
        "B_wq": _B_in["wq"],
        "B_wo": _B_in["wo"],
        "gate_w": _w((lambda *s: torch.randn(*s, dtype=torch.bfloat16) * 0.1)(512) * 16.0),
    }

def reference(A_x, A_k_cache, A_v_cache, A_wq, A_wo, B_x, B_k_cache, B_v_cache, B_wq, B_wo, gate_w):
    def _A_ref(x, k_cache, v_cache, wq, wo):
        B, Sq, D = x.shape
        H, Dh = 32, 128
        q = (lambda x, *d: x.permute(*d))((x @ wq).reshape(B, Sq, H, Dh), 0, 2, 1, 3)
        scores = torch.einsum("bhqd,bhkd->bhqk", q, k_cache) * (Dh ** -0.5)
        p = _softmax(scores, axis=-1)
        o = torch.einsum("bhqk,bhkd->bhqd", p, v_cache)
        return (lambda x, *d: x.permute(*d))(o, 0, 2, 1, 3).reshape(B, Sq, H * Dh) @ wo

    def _B_ref(x, k_cache, v_cache, wq, wo):
        B, Sq, D = x.shape
        Hq, Dh = 32, 128
        Hkv, Sk = k_cache.shape[1], k_cache.shape[2]
        W = min(4096, max(256, Sk // 4))
        rep = Hq // Hkv
        q = (lambda x, *d: x.permute(*d))((x @ wq).reshape(B, Sq, Hq, Dh), 0, 2, 1, 3)
        k = (lambda x, n, axis: torch.repeat_interleave(x, n, dim=axis))(k_cache[:, :, Sk - W:, :], rep, axis=1)
        v = (lambda x, n, axis: torch.repeat_interleave(x, n, dim=axis))(v_cache[:, :, Sk - W:, :], rep, axis=1)
        scores = torch.einsum("bhqd,bhkd->bhqk", q, k) * (Dh ** -0.5)
        p = _softmax(scores, axis=-1)
        o = torch.einsum("bhqk,bhkd->bhqd", p, v)
        return (lambda x, *d: x.permute(*d))(o, 0, 2, 1, 3).reshape(B, Sq, Hq * Dh) @ wo

    x = A_x
    g = _sigmoid((x * gate_w).sum(-1, keepdims=True))
    a = _A_ref(x, A_k_cache, A_v_cache, A_wq, A_wo)
    b = _B_ref(x, B_k_cache, B_v_cache, B_wq, B_wo)
    return g * a + (1 - g) * b
```

```cpp
#include <hip/hip_runtime.h>
#include <hip/hip_bf16.h>
#include <stdint.h>

#define RING 8

namespace {

constexpr int NB = 4, DM = 512, NH = 32, SK = 4096, DH = 128, WIN = 1024;
constexpr int HD = NH * DH;

using u32x4 = __attribute__((ext_vector_type(4))) unsigned;
using f32x4 = __attribute__((ext_vector_type(4))) float;
using bf16x2 = __attribute__((ext_vector_type(2))) __bf16;
using rsrc_t = __amdgpu_buffer_rsrc_t;

__device__ __forceinline__ float dot2(unsigned k, unsigned q, float acc) {
  return __builtin_amdgcn_fdot2_f32_bf16(__builtin_bit_cast(bf16x2, k), __builtin_bit_cast(bf16x2, q), acc, false);
}
__device__ __forceinline__ unsigned cvtpk(float lo, float hi) {
  unsigned r;
  asm volatile("v_cvt_pk_bf16_f32 %0, %1, %2" : "=v"(r) : "v"(lo), "v"(hi));
  return r;
}
template <int CTRL>
__device__ __forceinline__ float dpp(float x) {
  return __builtin_bit_cast(float, __builtin_amdgcn_mov_dpp(__builtin_bit_cast(int, x), CTRL, 0xf, 0xf, true));
}
constexpr int XOR1 = 0xB1, XOR2 = 0x4E, XOR7 = 0x141, XOR8 = 0x128;
constexpr int BCAST = 0x150;

__device__ __forceinline__ float bf_lo(unsigned w) { return __builtin_bit_cast(float, w << 16); }
__device__ __forceinline__ float bf_hi(unsigned w) { return __builtin_bit_cast(float, w & 0xffff0000u); }

__device__ __forceinline__ float xrow_sum(float x) {
  auto s = __builtin_amdgcn_permlane16_swap(__float_as_uint(x), __float_as_uint(x), false, false);
  x = __uint_as_float(s[0]) + __uint_as_float(s[1]);
  auto t = __builtin_amdgcn_permlane32_swap(__float_as_uint(x), __float_as_uint(x), false, false);
  return __uint_as_float(t[0]) + __uint_as_float(t[1]);
}
__device__ __forceinline__ float xrow_max(float x) {
  auto s = __builtin_amdgcn_permlane16_swap(__float_as_uint(x), __float_as_uint(x), false, false);
  x = fmaxf(__uint_as_float(s[0]), __uint_as_float(s[1]));
  auto t = __builtin_amdgcn_permlane32_swap(__float_as_uint(x), __float_as_uint(x), false, false);
  return fmaxf(__uint_as_float(t[0]), __uint_as_float(t[1]));
}
__device__ __forceinline__ float row_max(float x) {
  x = fmaxf(x, dpp<XOR8>(x));
  x = fmaxf(x, dpp<XOR7>(x));
  x = fmaxf(x, dpp<XOR2>(x));
  x = fmaxf(x, dpp<XOR1>(x));
  return x;
}
__device__ __forceinline__ float row_sum(float x) {
  x += dpp<XOR8>(x);
  x += dpp<XOR7>(x);
  x += dpp<XOR2>(x);
  x += dpp<XOR1>(x);
  return x;
}
#define PIN() __builtin_amdgcn_sched_barrier(0)

constexpr int NW = 8;
constexpr int NT = NW * 64;
constexpr int NL = 16;
constexpr int R = RING;
constexpr int NSEG = 5;
constexpr int LTOT = NSEG * 32;
constexpr float C2 = 0.08838834764831845f * 1.4426950408889634f;
constexpr unsigned KVBYTES = (unsigned)NB * NH * SK * DH * 2;
constexpr unsigned WBYTES = (unsigned)DM * HD * 2;

struct Params {
  const uint16_t* x;
  const uint16_t* kA; const uint16_t* vA; const uint16_t* wqA; const uint16_t* woA;
  const uint16_t* kB; const uint16_t* vB; const uint16_t* wqB; const uint16_t* woB;
  const uint16_t* gate;
  float* y;
  float* ml;
  __hip_bfloat16* out;
};

constexpr int L_QP = 0;
constexpr int REC = 132;
constexpr int L_REC = L_QP + NW * 128;
constexpr int L_O = L_REC + NW * REC;
constexpr int L_END = L_O + 128;

struct Ctx {
  rsrc_t kA, vA, kB, vB;
  unsigned offA, offB;
  unsigned vo;
};

template <int L>
__device__ __forceinline__ u32x4 ld_stream(const Ctx& c) {
  constexpr int S = L / 32, PH = (L / 16) & 1, N = L % 16;
  constexpr unsigned rel = (N / 4) * 32768u + (S > 0 ? (S - 1) * 131072u : 0u);
  const unsigned so = (S == 0 ? c.offB : c.offA) + rel;
  const rsrc_t& r = S == 0 ? (PH ? c.vB : c.kB) : (PH ? c.vA : c.kA);
  return __builtin_amdgcn_raw_buffer_load_b128(r, c.vo + (N % 4) * 1024, so, 2  );
}

__device__ __forceinline__ u32x4 ld_w(const rsrc_t& r, unsigned vo, unsigned so) {
  return __builtin_amdgcn_raw_buffer_load_b128(r, vo, so, 0);
}

__device__ __forceinline__ void q_partial(const u32x4 (&W)[NL], const u32x4 (&xv)[2], float* lds, int wave, int r, int c) {
  float qa[8];
#pragma unroll
  for (int k = 0; k < 8; ++k) qa[k] = 0.f;
#pragma unroll
  for (int i = 0; i < NL; ++i) {
    const unsigned xw = xv[i >> 3][(i >> 1) & 3];
    const float xf = (i & 1) ? bf_hi(xw) : bf_lo(xw);
#pragma unroll
    for (int k = 0; k < 4; ++k) {
      qa[2 * k] = fmaf(xf, bf_lo(W[i][k]), qa[2 * k]);
      qa[2 * k + 1] = fmaf(xf, bf_hi(W[i][k]), qa[2 * k + 1]);
    }
  }
#pragma unroll
  for (int k = 0; k < 8; ++k) qa[k] = xrow_sum(qa[k]);
  if (r == 0) {
    float* dst = lds + L_QP + wave * 128 + 8 * c;
    *reinterpret_cast<f32x4*>(dst) = f32x4{qa[0], qa[1], qa[2], qa[3]};
    *reinterpret_cast<f32x4*>(dst + 4) = f32x4{qa[4], qa[5], qa[6], qa[7]};
  }
}
__device__ __forceinline__ u32x4 q_gather(const float* lds, int c) {
  f32x4 a = {0.f, 0.f, 0.f, 0.f}, b = {0.f, 0.f, 0.f, 0.f};
#pragma unroll
  for (int w = 0; w < NW; ++w) {
    a += *reinterpret_cast<const f32x4*>(lds + L_QP + w * 128 + 8 * c);
    b += *reinterpret_cast<const f32x4*>(lds + L_QP + w * 128 + 8 * c + 4);
  }
  return u32x4{cvtpk(a[0], a[1]), cvtpk(a[2], a[3]), cvtpk(b[0], b[1]), cvtpk(b[2], b[3])};
}

struct State { float m, l, acc[8]; };

__device__ __forceinline__ float qk(const u32x4& k, const u32x4& q) {
  float d = dot2(k[0], q[0], 0.f);
  d = dot2(k[1], q[1], d);
  d = dot2(k[2], q[2], d);
  return dot2(k[3], q[3], d);
}

__device__ __forceinline__ float softmax_step(State& st, float (&x)[NL], int c) {
#define TR_STEP(HALF, CTRL, BIT)                                          \
  _Pragma("unroll") for (int h = 0; h < (HALF); ++h) {                     \
    const float lo_ = x[h], hi_ = x[h + (HALF)];                           \
    const float t1_ = lo_ + dpp<CTRL>(lo_), t2_ = hi_ + dpp<CTRL>(hi_);    \
    x[h] = (c & (BIT)) ? t2_ : t1_;                                        \
  }
  TR_STEP(8, XOR8, 8) TR_STEP(4, XOR7, 4) TR_STEP(2, XOR2, 2) TR_STEP(1, XOR1, 1)
#undef TR_STEP
  const float s = x[0] * C2;
  const float mn = fmaxf(st.m, row_max(s));
  const float alpha = __builtin_amdgcn_exp2f(st.m - mn);
  const float p = __builtin_amdgcn_exp2f(s - mn);
  st.l = fmaf(st.l, alpha, p);
  st.m = mn;
#pragma unroll
  for (int k = 0; k < 8; ++k) st.acc[k] *= alpha;
  return p;
}

template <int I>
__device__ __forceinline__ void pv_step(State& st, float p, const u32x4& v) {
  const float pb = dpp<BCAST + I>(p);
#pragma unroll
  for (int k = 0; k < 4; ++k) {
    st.acc[2 * k] = fmaf(pb, bf_lo(v[k]), st.acc[2 * k]);
    st.acc[2 * k + 1] = fmaf(pb, bf_hi(v[k]), st.acc[2 * k + 1]);
  }
}

template <int S, int N, bool TR>
struct KPhase {
  static __device__ __forceinline__ void run(const Ctx& c, u32x4 (&ring)[R], const u32x4& q, float (&x)[NL], u32x4 (&W)[NL],
                                             const rsrc_t& rw, unsigned wvo) {
    constexpr int L = S * 32 + N;
    x[N] = qk(ring[L % R], q);
    PIN();
    ring[L % R] = ld_stream<L + R>(c);
    if constexpr (TR) W[N] = ld_w(rw, wvo, N * 8 * DM * 2);
    PIN();
    KPhase<S, N + 1, TR>::run(c, ring, q, x, W, rw, wvo);
  }
};
template <int S, bool TR>
struct KPhase<S, NL, TR> {
  static __device__ __forceinline__ void run(const Ctx&, u32x4 (&)[R], const u32x4&, float (&)[NL], u32x4 (&)[NL], const rsrc_t&, unsigned) {}
};
template <int S, int N>
struct VPhase {
  static __device__ __forceinline__ void run(const Ctx& c, u32x4 (&ring)[R], State& st, float p) {
    constexpr int L = S * 32 + 16 + N;
    pv_step<N>(st, p, ring[L % R]);
    PIN();
    if constexpr (L + R < LTOT) ring[L % R] = ld_stream<L + R>(c);
    PIN();
    VPhase<S, N + 1>::run(c, ring, st, p);
  }
};
template <int S>
struct VPhase<S, NL> {
  static __device__ __forceinline__ void run(const Ctx&, u32x4 (&)[R], State&, float) {}
};

__device__ __forceinline__ void wave_record(State& st, float* lds, int wave, int r, int c) {
  const float M = xrow_max(st.m);
  const float w = __builtin_amdgcn_exp2f(st.m - M);
  const float L = xrow_sum(row_sum(st.l) * w);
  float a[8];
#pragma unroll
  for (int k = 0; k < 8; ++k) a[k] = xrow_sum(st.acc[k] * w);
  if (r == 0) {
    float* rec = lds + L_REC + wave * REC;
    *reinterpret_cast<f32x4*>(rec + 8 * c) = f32x4{a[0], a[1], a[2], a[3]};
    *reinterpret_cast<f32x4*>(rec + 8 * c + 4) = f32x4{a[4], a[5], a[6], a[7]};
    if (c == 0) { rec[128] = M; rec[129] = L; }
  }
}
__device__ __forceinline__ void wg_merge(float* lds, int tid, float* ml_out) {
  if (tid < 128) {
    float M = lds[L_REC + 128];
#pragma unroll
    for (int w = 1; w < NW; ++w) M = fmaxf(M, lds[L_REC + w * REC + 128]);
    float o = 0.f, L = 0.f;
#pragma unroll
    for (int w = 0; w < NW; ++w) {
      const float wt = __builtin_amdgcn_exp2f(lds[L_REC + w * REC + 128] - M);
      o = fmaf(wt, lds[L_REC + w * REC + tid], o);
      L = fmaf(wt, lds[L_REC + w * REC + 129], L);
    }
    lds[L_O + (tid & 7) * 16 + (tid >> 3)] = o;
    if (tid == 0) *reinterpret_cast<float2*>(ml_out) = make_float2(M, L);
  }
}
__device__ __forceinline__ void out_proj(const u32x4 (&W)[NL], const float* lds, int wave, int lane, float* y_out) {
  const int r8 = lane >> 3, c8 = lane & 7;
  f32x4 o4[4];
#pragma unroll
  for (int j = 0; j < 4; ++j) o4[j] = *reinterpret_cast<const f32x4*>(lds + L_O + r8 * 16 + 4 * j);
  float y[8];
#pragma unroll
  for (int k = 0; k < 8; ++k) y[k] = 0.f;
#pragma unroll
  for (int i = 0; i < NL; ++i) {
    const float o = o4[i >> 2][i & 3];
#pragma unroll
    for (int k = 0; k < 4; ++k) {
      y[2 * k] = fmaf(o, bf_lo(W[i][k]), y[2 * k]);
      y[2 * k + 1] = fmaf(o, bf_hi(W[i][k]), y[2 * k + 1]);
    }
  }
#pragma unroll
  for (int k = 0; k < 8; ++k) { y[k] += dpp<XOR8>(y[k]); y[k] = xrow_sum(y[k]); }
  if (lane < 8) {
    float* dst = y_out + 64 * wave + 8 * c8;
    *reinterpret_cast<f32x4*>(dst) = f32x4{y[0], y[1], y[2], y[3]};
    *reinterpret_cast<f32x4*>(dst + 4) = f32x4{y[4], y[5], y[6], y[7]};
  }
}

__device__ __forceinline__ rsrc_t mk(const void* p, unsigned bytes) {
  return __builtin_amdgcn_make_buffer_rsrc(const_cast<void*>(p), 0, bytes, 0x00020000);
}

template <int N>
struct Prologue {
  static __device__ __forceinline__ void run(const Ctx& c, u32x4 (&W)[NL], u32x4 (&K0)[NL], const rsrc_t& rq, unsigned qvo) {
    W[2 * N] = ld_w(rq, qvo, (2 * N) * HD * 2);
    W[2 * N + 1] = ld_w(rq, qvo, (2 * N + 1) * HD * 2);
    K0[N] = ld_stream<N>(c);
    PIN();
    Prologue<N + 1>::run(c, W, K0, rq, qvo);
  }
};
template <>
struct Prologue<8> {
  static __device__ __forceinline__ void run(const Ctx&, u32x4 (&)[NL], u32x4 (&)[NL], const rsrc_t&, unsigned) {}
};
template <int N, int E>
struct LoadK0 {
  static __device__ __forceinline__ void run(const Ctx& c, u32x4 (&K0)[NL]) { K0[N] = ld_stream<N>(c); LoadK0<N + 1, E>::run(c, K0); }
};
template <int E>
struct LoadK0<E, E> { static __device__ __forceinline__ void run(const Ctx&, u32x4 (&)[NL]) {} };
template <int N>
struct LoadRing {
  static __device__ __forceinline__ void run(const Ctx& c, u32x4 (&ring)[R]) { ring[(16 + N) % R] = ld_stream<16 + N>(c); LoadRing<N + 1>::run(c, ring); }
};
template <>
struct LoadRing<R> { static __device__ __forceinline__ void run(const Ctx&, u32x4 (&)[R]) {} };

__device__ __forceinline__ void reset(State& st) {
  st.m = -INFINITY; st.l = 0.f;
#pragma unroll
  for (int k = 0; k < 8; ++k) st.acc[k] = 0.f;
}

__global__ void __launch_bounds__(NT, 2) attn_partial(Params p) {
  __shared__ __attribute__((aligned(16))) float lds[L_END];
  const int bid = blockIdx.x;
  const int h = (bid & 7) * 4 + ((bid >> 3) & 3);
  const int j = bid >> 5;
  const int b = j >> 1, half = j & 1;
  const int tid = threadIdx.x, lane = tid & 63;
  const int wave = __builtin_amdgcn_readfirstlane(tid >> 6);
  const int r = lane >> 4, c = lane & 15;

  Ctx cx;
  cx.kA = mk(p.kA, KVBYTES); cx.vA = mk(p.vA, KVBYTES); cx.kB = mk(p.kB, KVBYTES); cx.vB = mk(p.vB, KVBYTES);
  const unsigned bh = (unsigned)(b * NH + h) * SK;
  cx.offB = (bh + (SK - WIN) + half * 512) * (DH * 2) + wave * 4096;
  cx.offA = (bh + half * 2048) * (DH * 2) + wave * 4096;
  cx.vo = lane * 16;
  const rsrc_t rqA = mk(p.wqA, WBYTES), rqB = mk(p.wqB, WBYTES), roA = mk(p.woA, WBYTES), roB = mk(p.woB, WBYTES);
  const unsigned qvo = ((64 * wave + 16 * r) * HD + h * DH + 8 * c) * 2;
  const unsigned ovo = ((h * DH + (lane >> 3)) * DM + 64 * wave + 8 * (lane & 7)) * 2;

  u32x4 W[NL], K0[NL], ring[R], xv[2];
  {
    const char* xp = reinterpret_cast<const char*>(p.x) + (b * DM + 64 * wave + 16 * r) * 2;
    xv[0] = *reinterpret_cast<const u32x4*>(xp);
    xv[1] = *reinterpret_cast<const u32x4*>(xp + 16);
  }
  PIN();
  Prologue<0>::run(cx, W, K0, rqB, qvo);
  LoadK0<8, 16>::run(cx, K0);
  LoadRing<0>::run(cx, ring);
  PIN();

  float* yb = p.y + (size_t)((b * NH + h) * 4 + half) * DM;
  float* mlb = p.ml + (size_t)((b * NH + h) * 4 + half) * 2;

  q_partial(W, xv, lds, wave, r, c);
  PIN();
#pragma unroll
  for (int i = 0; i < NL; ++i) W[i] = ld_w(rqA, qvo, i * HD * 2);
  PIN();
  __syncthreads();
  u32x4 q = q_gather(lds, c);

  State st;
  float x[NL], pr;
  reset(st);
#pragma unroll
  for (int i = 0; i < NL; ++i) x[i] = qk(K0[i], q);
  pr = softmax_step(st, x, c);
  PIN();
  VPhase<0, 0>::run(cx, ring, st, pr);
  wave_record(st, lds, wave, r, c);
  __syncthreads();
  q_partial(W, xv, lds, wave, r, c);
  wg_merge(lds, tid, mlb + 4);
  __syncthreads();
  q = q_gather(lds, c);
  reset(st);
  PIN();

  KPhase<1, 0, true>::run(cx, ring, q, x, W, roB, ovo);
  pr = softmax_step(st, x, c);
  PIN();
  VPhase<1, 0>::run(cx, ring, st, pr);
  out_proj(W, lds, wave, lane, yb + 2 * DM);
  PIN();
  KPhase<2, 0, true>::run(cx, ring, q, x, W, roA, ovo);
  pr = softmax_step(st, x, c);
  PIN();
  VPhase<2, 0>::run(cx, ring, st, pr);
  KPhase<3, 0, false>::run(cx, ring, q, x, W, roA, ovo);
  pr = softmax_step(st, x, c);
  PIN();
  VPhase<3, 0>::run(cx, ring, st, pr);
  KPhase<4, 0, false>::run(cx, ring, q, x, W, roA, ovo);
  pr = softmax_step(st, x, c);
  PIN();
  VPhase<4, 0>::run(cx, ring, st, pr);

  wave_record(st, lds, wave, r, c);
  __syncthreads();
  wg_merge(lds, tid, mlb);
  __syncthreads();
  out_proj(W, lds, wave, lane, yb);
}

__global__ void __launch_bounds__(256) combine(Params p) {
  __shared__ __attribute__((aligned(16))) float red[4][32];
  __shared__ float gpart[4];
  const int b = blockIdx.x >> 4, cc = blockIdx.x & 15;
  const int tid = threadIdx.x, lane = tid & 63, wave = tid >> 6;
  const int h = tid >> 3, c4 = tid & 7;
  const float* ml = p.ml + (size_t)(b * NH + h) * 8;
  const f32x4 mlA = *reinterpret_cast<const f32x4*>(ml);
  const f32x4 mlB = *reinterpret_cast<const f32x4*>(ml + 4);
  const float* y = p.y + (size_t)(b * NH + h) * 4 * DM + cc * 32 + c4 * 4;
  const f32x4 y0 = *reinterpret_cast<const f32x4*>(y);
  const f32x4 y1 = *reinterpret_cast<const f32x4*>(y + DM);
  const f32x4 y2 = *reinterpret_cast<const f32x4*>(y + 2 * DM);
  const f32x4 y3 = *reinterpret_cast<const f32x4*>(y + 3 * DM);
  const unsigned xw = reinterpret_cast<const unsigned*>(p.x)[b * (DM / 2) + tid];
  const unsigned gw = reinterpret_cast<const unsigned*>(p.gate)[tid];
  float gs = bf_lo(xw) * bf_lo(gw) + bf_hi(xw) * bf_hi(gw);
  gs = xrow_sum(row_sum(gs));
  if (lane == 0) gpart[wave] = gs;

  const float MA = fmaxf(mlA[0], mlA[2]), MB = fmaxf(mlB[0], mlB[2]);
  const float a0 = __builtin_amdgcn_exp2f(mlA[0] - MA), a1 = __builtin_amdgcn_exp2f(mlA[2] - MA);
  const float b0 = __builtin_amdgcn_exp2f(mlB[0] - MB), b1 = __builtin_amdgcn_exp2f(mlB[2] - MB);
  const float iA = 1.f / (a0 * mlA[1] + a1 * mlA[3]);
  const float iB = 1.f / (b0 * mlB[1] + b1 * mlB[3]);
  f32x4 sa = (y0 * a0 + y1 * a1) * iA;
  f32x4 sb = (y2 * b0 + y3 * b1) * iB;
  __syncthreads();
  const float z = gpart[0] + gpart[1] + gpart[2] + gpart[3];
  const float g = 1.f / (1.f + __expf(-z));
  f32x4 s = sa * g + sb * (1.f - g);
#pragma unroll
  for (int k = 0; k < 4; ++k) { float t = s[k]; t += dpp<XOR8>(t); s[k] = xrow_sum(t); }
  if (lane < 8) *reinterpret_cast<f32x4*>(&red[wave][4 * lane]) = s;
  __syncthreads();
  if (tid < 32) {
    const float v = red[0][tid] + red[1][tid] + red[2][tid] + red[3][tid];
    p.out[b * DM + cc * 32 + tid] = __float2bfloat16(v);
  }
}

}

extern "C" void kernel_launch(void* const* d_in, const int* in_sizes, int n_in, void* d_out, int out_size, void* d_ws,
                              size_t ws_size, hipStream_t stream) {
  Params p;
  p.x = (const uint16_t*)d_in[0];
  p.kA = (const uint16_t*)d_in[1];
  p.vA = (const uint16_t*)d_in[2];
  p.wqA = (const uint16_t*)d_in[3];
  p.woA = (const uint16_t*)d_in[4];
  p.kB = (const uint16_t*)d_in[6];
  p.vB = (const uint16_t*)d_in[7];
  p.wqB = (const uint16_t*)d_in[8];
  p.woB = (const uint16_t*)d_in[9];
  p.gate = (const uint16_t*)d_in[10];
  p.y = (float*)d_ws;
  p.ml = (float*)((char*)d_ws + (size_t)NB * NH * 4 * DM * 4);
  p.out = (__hip_bfloat16*)d_out;
  attn_partial<<<256, NT, 0, stream>>>(p);
  combine<<<64, 256, 0, stream>>>(p);
}
```

```cpp
#include <hip/hip_runtime.h>
#include <hip/hip_bf16.h>
#include <stdint.h>

#define RING 4
#define WAUX 2
#define PWIN 4

#define STAMP(k)
#define STAMP_DECL
#define STAMP_FLUSH(a, b)

namespace {

constexpr int NB = 4, DM = 512, NH = 32, SK = 4096, DH = 128, WIN = 1024;
constexpr int HD = NH * DH;

using u32x4 = __attribute__((ext_vector_type(4))) unsigned;
using f32x4 = __attribute__((ext_vector_type(4))) float;
using f32x2 = __attribute__((ext_vector_type(2))) float;
using bf16x2 = __attribute__((ext_vector_type(2))) __bf16;
using rsrc_t = __amdgpu_buffer_rsrc_t;

__device__ __forceinline__ float dot2(unsigned k, unsigned q, float acc) {
  return __builtin_amdgcn_fdot2_f32_bf16(__builtin_bit_cast(bf16x2, k), __builtin_bit_cast(bf16x2, q), acc, false);
}
__device__ __forceinline__ unsigned cvtpk(float lo, float hi) {
  unsigned r;
  asm volatile("v_cvt_pk_bf16_f32 %0, %1, %2" : "=v"(r) : "v"(lo), "v"(hi));
  return r;
}
template <int CTRL>
__device__ __forceinline__ float dpp(float x) {
  return __builtin_bit_cast(float, __builtin_amdgcn_mov_dpp(__builtin_bit_cast(int, x), CTRL, 0xf, 0xf, true));
}
constexpr int XOR1 = 0xB1, XOR2 = 0x4E, XOR7 = 0x141, XOR8 = 0x128;
constexpr int BCAST = 0x150;

__device__ __forceinline__ float bf_lo(unsigned w) { return __builtin_bit_cast(float, w << 16); }
__device__ __forceinline__ float bf_hi(unsigned w) { return __builtin_bit_cast(float, w & 0xffff0000u); }
__device__ __forceinline__ float rbf(float v) { return bf_lo(cvtpk(v, v)); }

__device__ __forceinline__ float tr32(float u, float w) {
  auto t = __builtin_amdgcn_permlane32_swap(__float_as_uint(u), __float_as_uint(w), false, false);
  return __uint_as_float(t[0]) + __uint_as_float(t[1]);
}
__device__ __forceinline__ float tr16(float u, float w) {
  auto t = __builtin_amdgcn_permlane16_swap(__float_as_uint(u), __float_as_uint(w), false, false);
  return __uint_as_float(t[0]) + __uint_as_float(t[1]);
}
__device__ __forceinline__ float xrow_sum(float x) { x = tr16(x, x); return tr32(x, x); }
__device__ __forceinline__ float xrow_max(float x) {
  auto s = __builtin_amdgcn_permlane16_swap(__float_as_uint(x), __float_as_uint(x), false, false);
  x = fmaxf(__uint_as_float(s[0]), __uint_as_float(s[1]));
  auto t = __builtin_amdgcn_permlane32_swap(__float_as_uint(x), __float_as_uint(x), false, false);
  return fmaxf(__uint_as_float(t[0]), __uint_as_float(t[1]));
}
__device__ __forceinline__ float row_max(float x) {
  x = fmaxf(x, dpp<XOR8>(x));
  x = fmaxf(x, dpp<XOR7>(x));
  x = fmaxf(x, dpp<XOR2>(x));
  x = fmaxf(x, dpp<XOR1>(x));
  return x;
}
__device__ __forceinline__ float row_sum(float x) {
  x += dpp<XOR8>(x);
  x += dpp<XOR7>(x);
  x += dpp<XOR2>(x);
  x += dpp<XOR1>(x);
  return x;
}
__device__ __forceinline__ f32x2 rows_reduce8(const float (&a)[8]) {
  const float u0 = tr32(a[0], a[4]), u1 = tr32(a[1], a[5]), u2 = tr32(a[2], a[6]), u3 = tr32(a[3], a[7]);
  return f32x2{tr16(u0, u2), tr16(u1, u3)};
}
#define PIN() __builtin_amdgcn_sched_barrier(0)

constexpr int NW = 8;
constexpr int NT = NW * 64;
constexpr int NL = 16;
constexpr int R = RING;
constexpr int NSEG = 5;
constexpr int LTOT = NSEG * 32;
constexpr float SCALE = 0.08838834764831845f, LOG2E = 1.4426950408889634f;
constexpr unsigned KVBYTES = (unsigned)NB * NH * SK * DH * 2;
constexpr unsigned WBYTES = (unsigned)DM * HD * 2;

struct Params {
  const uint16_t* x;
  const uint16_t* kA; const uint16_t* vA; const uint16_t* wqA; const uint16_t* woA;
  const uint16_t* kB; const uint16_t* vB; const uint16_t* wqB; const uint16_t* woB;
  const uint16_t* gate;
  float* y;
  float* ml;
  __hip_bfloat16* out;
};

constexpr int L_QP = 0;
constexpr int REC = 132;
constexpr int L_REC = L_QP + 2 * NW * 128;
constexpr int L_O = L_REC + 2 * NW * REC;
constexpr int L_CNT = L_O + 2 * 64;
constexpr int L_END = L_CNT + 4;

struct Ctx {
  rsrc_t kA, vA, kB, vB;
  unsigned offA, offB;
  unsigned vo;
};

template <int L>
__device__ __forceinline__ u32x4 ld_stream(const Ctx& c) {
  constexpr int S = L / 32, PH = (L / 16) & 1, N = L % 16;
  constexpr unsigned rel = (N / 4) * 32768u + (S > 0 ? (S - 1) * 131072u : 0u);
  const unsigned so = (S == 0 ? c.offB : c.offA) + rel;
  const rsrc_t& r = S == 0 ? (PH ? c.vB : c.kB) : (PH ? c.vA : c.kA);
  return __builtin_amdgcn_raw_buffer_load_b128(r, c.vo + (N % 4) * 1024, so, 2  );
}
__device__ __forceinline__ u32x4 ld_w(const rsrc_t& r, unsigned vo, unsigned so) {
  return __builtin_amdgcn_raw_buffer_load_b128(r, vo, so, WAUX);
}

template <int I>
__device__ __forceinline__ void q_step(float (&qa)[8], const u32x4& w, const u32x4 (&xv)[2]) {
  const unsigned xw = xv[I >> 3][(I >> 1) & 3];
  const float xf = (I & 1) ? bf_hi(xw) : bf_lo(xw);
#pragma unroll
  for (int k = 0; k < 4; ++k) {
    qa[2 * k] = fmaf(xf, bf_lo(w[k]), qa[2 * k]);
    qa[2 * k + 1] = fmaf(xf, bf_hi(w[k]), qa[2 * k + 1]);
  }
}
__device__ __forceinline__ void q_post(const float (&qa)[8], float* lds, int wave, int r, int c) {
  *reinterpret_cast<f32x2*>(lds + wave * 128 + 8 * c + 2 * r) = rows_reduce8(qa);
}
__device__ __forceinline__ u32x4 q_gather(const float* lds, int c) {
  f32x4 a = {0.f, 0.f, 0.f, 0.f}, b = {0.f, 0.f, 0.f, 0.f};
#pragma unroll
  for (int w = 0; w < NW; ++w) {
    a += *reinterpret_cast<const f32x4*>(lds + w * 128 + 8 * c);
    b += *reinterpret_cast<const f32x4*>(lds + w * 128 + 8 * c + 4);
  }
  return u32x4{cvtpk(a[0], a[1]), cvtpk(a[2], a[3]), cvtpk(b[0], b[1]), cvtpk(b[2], b[3])};
}

struct State { float m, l, acc[8]; };

__device__ __forceinline__ float qk(const u32x4& k, const u32x4& q) {
  float d = dot2(k[0], q[0], 0.f);
  d = dot2(k[1], q[1], d);
  d = dot2(k[2], q[2], d);
  return dot2(k[3], q[3], d);
}

__device__ __forceinline__ float softmax_step(State& st, float (&x)[NL], int c) {
#define TR_STEP(HALF, CTRL, BIT)                                          \
  _Pragma("unroll") for (int h = 0; h < (HALF); ++h) {                     \
    const float lo_ = x[h], hi_ = x[h + (HALF)];                           \
    const float t1_ = lo_ + dpp<CTRL>(lo_), t2_ = hi_ + dpp<CTRL>(hi_);    \
    x[h] = (c & (BIT)) ? t2_ : t1_;                                        \
  }
  TR_STEP(8, XOR8, 8) TR_STEP(4, XOR7, 4) TR_STEP(2, XOR2, 2) TR_STEP(1, XOR1, 1)
#undef TR_STEP
  const float s = rbf(rbf(x[0]) * SCALE) * LOG2E;
  const float mn = fmaxf(st.m, row_max(s));
  const float alpha = __builtin_amdgcn_exp2f(st.m - mn);
  const float p = __builtin_amdgcn_exp2f(s - mn);
  st.l = fmaf(st.l, alpha, p);
  st.m = mn;
#pragma unroll
  for (int k = 0; k < 8; ++k) st.acc[k] *= alpha;
  return p;
}

template <int I>
__device__ __forceinline__ void pv_step(State& st, float p, const u32x4& v) {
  const float pb = dpp<BCAST + I>(p);
#pragma unroll
  for (int k = 0; k < 4; ++k) {
    st.acc[2 * k] = fmaf(pb, bf_lo(v[k]), st.acc[2 * k]);
    st.acc[2 * k + 1] = fmaf(pb, bf_hi(v[k]), st.acc[2 * k + 1]);
  }
}

template <int S, int N, int TR0>
struct KPhase {
  static __device__ __forceinline__ void run(const Ctx& c, u32x4 (&ring)[R], const u32x4& q, float (&x)[NL], u32x4 (&W)[NL],
                                             const rsrc_t& rw, unsigned wvo) {
    constexpr int L = S * 32 + N;
    x[N] = qk(ring[L % R], q);
    PIN();
    ring[L % R] = ld_stream<L + R>(c);
    if constexpr (TR0 >= 0 && (N & 1)) W[TR0 + N / 2] = ld_w(rw, wvo, (TR0 + N / 2) * 8 * DM * 2);
    PIN();
    KPhase<S, N + 1, TR0>::run(c, ring, q, x, W, rw, wvo);
  }
};
template <int S, int TR0>
struct KPhase<S, NL, TR0> {
  static __device__ __forceinline__ void run(const Ctx&, u32x4 (&)[R], const u32x4&, float (&)[NL], u32x4 (&)[NL], const rsrc_t&, unsigned) {}
};
template <int S, int N, int TR0>
struct VPhase {
  static __device__ __forceinline__ void run(const Ctx& c, u32x4 (&ring)[R], State& st, float p, u32x4 (&W)[NL], const rsrc_t& rw,
                                             unsigned wvo) {
    constexpr int L = S * 32 + 16 + N;
    pv_step<N>(st, p, ring[L % R]);
    PIN();
    if constexpr (L + R < LTOT) ring[L % R] = ld_stream<L + R>(c);
    if constexpr (TR0 >= 0 && (N & 1)) W[TR0 + N / 2] = ld_w(rw, wvo, (TR0 + N / 2) * 8 * DM * 2);
    PIN();
    VPhase<S, N + 1, TR0>::run(c, ring, st, p, W, rw, wvo);
  }
};
template <int S, int TR0>
struct VPhase<S, NL, TR0> {
  static __device__ __forceinline__ void run(const Ctx&, u32x4 (&)[R], State&, float, u32x4 (&)[NL], const rsrc_t&, unsigned) {}
};

__device__ __forceinline__ void wave_record(State& st, float* lds, int wave, int r, int c, int lane) {
  const float M = xrow_max(st.m);
  const float w = __builtin_amdgcn_exp2f(st.m - M);
  const float L = xrow_sum(row_sum(st.l) * w);
  float a[8];
#pragma unroll
  for (int k = 0; k < 8; ++k) a[k] = st.acc[k] * w;
  float* rec = lds + wave * REC;
  *reinterpret_cast<f32x2*>(rec + 8 * c + 2 * r) = rows_reduce8(a);
  if (lane == 0) *reinterpret_cast<f32x2*>(rec + 128) = f32x2{M, L};
}
__device__ __forceinline__ void wg_merge(float* lds, int tid, float* ml_out) {
  if (tid < 128) {
    const int br = tid >> 6, d = ((tid >> 3) & 7) + 16 * (tid & 7);
    const float* rec = lds + L_REC + br * NW * REC;
    float M = rec[128];
#pragma unroll
    for (int w = 1; w < NW; ++w) M = fmaxf(M, rec[w * REC + 128]);
    float o0 = 0.f, o1 = 0.f, L = 0.f;
#pragma unroll
    for (int w = 0; w < NW; ++w) {
      const float wt = __builtin_amdgcn_exp2f(rec[w * REC + 128] - M);
      o0 = fmaf(wt, rec[w * REC + d], o0);
      o1 = fmaf(wt, rec[w * REC + d + 8], o1);
      L = fmaf(wt, rec[w * REC + 129], L);
    }
    reinterpret_cast<unsigned*>(lds)[L_O + tid] = cvtpk(o0, o1);
    if ((tid & 63) == 0) *reinterpret_cast<float2*>(ml_out + 4 * br) = make_float2(M, L);
  }
}
__device__ __forceinline__ void wo_pair(u32x4 (&W)[NL]) {
#pragma unroll
  for (int j = 0; j < NL / 2; ++j)
#pragma unroll
    for (int k = 0; k < 4; ++k) {
      const unsigned a = W[2 * j][k], b = W[2 * j + 1][k];
      W[2 * j][k] = __builtin_amdgcn_perm(b, a, 0x05040100u);
      W[2 * j + 1][k] = __builtin_amdgcn_perm(b, a, 0x07060302u);
    }
}
__device__ __forceinline__ void out_proj(const u32x4 (&W)[NL], const float* lds, int wave, int lane, float* y_out) {
  const int r8 = lane >> 3, c8 = lane & 7;
  const u32x4 oa = *reinterpret_cast<const u32x4*>(lds + r8 * 8), ob = *reinterpret_cast<const u32x4*>(lds + r8 * 8 + 4);
  float y[8];
#pragma unroll
  for (int k = 0; k < 8; ++k) y[k] = 0.f;
#pragma unroll
  for (int j = 0; j < NL / 2; ++j) {
    const unsigned o = j < 4 ? oa[j & 3] : ob[j & 3];
#pragma unroll
    for (int k = 0; k < 4; ++k) {
      y[2 * k] = dot2(W[2 * j][k], o, y[2 * k]);
      y[2 * k + 1] = dot2(W[2 * j + 1][k], o, y[2 * k + 1]);
    }
  }
  const f32x2 t = rows_reduce8(y);
  const float t0 = t[0] + dpp<XOR8>(t[0]), t1 = t[1] + dpp<XOR8>(t[1]);
  y_out[64 * wave + 8 * c8 + r8] = (lane & 8) ? t1 : t0;
}

__device__ __forceinline__ rsrc_t mk(const void* p, unsigned bytes) {
  return __builtin_amdgcn_make_buffer_rsrc(const_cast<void*>(p), 0, bytes, 0x00020000);
}

struct Pro {
  const Ctx& c; u32x4 (&WA)[NL]; u32x4 (&WB)[NL]; u32x4 (&K0)[NL];
  const rsrc_t& rqA; const rsrc_t& rqB; unsigned qvo;
  template <int P>
  __device__ __forceinline__ void issue() {
    if constexpr (P < 32) {
      if constexpr ((P & 1) == 0) WB[P / 2] = ld_w(rqB, qvo, (P / 2) * HD * 2);
      else K0[P / 2] = ld_stream<P / 2>(c);
    } else if constexpr (P < 56) {
      constexpr int T = (P - 32) / 3, U = (P - 32) % 3;
      if constexpr (U < 2) WA[2 * T + U] = ld_w(rqA, qvo, (2 * T + U) * HD * 2);
      else WB[T] = ld_stream<16 + T>(c);
    } else if constexpr (P < 64) {
      WB[P - 48] = ld_stream<16 + P - 48>(c);
    }
  }
  template <int P0, int P1>
  __device__ __forceinline__ void range() {
    if constexpr (P0 < P1) { issue<P0>(); range<P0 + 1, P1>(); }
  }
};
template <int I>
__device__ __forceinline__ void qb_loop(Pro& pro, float (&qa)[8], const u32x4 (&xv)[2]) {
  if constexpr (I < NL) {
    q_step<I>(qa, pro.WB[I], xv);
    PIN();
    pro.template range<2 * (I + PWIN), 2 * (I + PWIN) + 2>();
    PIN();
    qb_loop<I + 1>(pro, qa, xv);
  }
}
template <int N>
__device__ __forceinline__ void v0_loop(const Ctx& c, Pro& pro, u32x4 (&ring)[R], State& st, float p, float (&qa)[8],
                                        const u32x4 (&xv)[2], float* lds, int wave, int r, int cc, int lane) {
  if constexpr (N < NL) {
    if constexpr (N < 8) { q_step<2 * N>(qa, pro.WA[2 * N], xv); q_step<2 * N + 1>(qa, pro.WA[2 * N + 1], xv); }
    pv_step<N>(st, p, pro.WB[N]);
    PIN();
    if constexpr (N == 7) {
      q_post(qa, lds + L_QP + NW * 128, wave, r, cc);
      __builtin_amdgcn_fence(__ATOMIC_RELEASE, "workgroup");
      if (lane == 0) __hip_atomic_fetch_add(reinterpret_cast<unsigned*>(lds) + L_CNT, 1u, __ATOMIC_RELAXED, __HIP_MEMORY_SCOPE_WORKGROUP);
    }
    if constexpr (N >= NL - R) ring[(32 + N - (NL - R)) % R] = ld_stream<32 + N - (NL - R)>(c);
    PIN();
    v0_loop<N + 1>(c, pro, ring, st, p, qa, xv, lds, wave, r, cc, lane);
  }
}

__device__ __forceinline__ void reset(State& st) {
  st.m = -INFINITY; st.l = 0.f;
#pragma unroll
  for (int k = 0; k < 8; ++k) st.acc[k] = 0.f;
}

__global__ void __launch_bounds__(NT, 2) attn_partial(Params p) {
  __shared__ __attribute__((aligned(16))) float lds[L_END];
  const int bid = blockIdx.x;
  STAMP_DECL;
  STAMP(0);
  const int h = (bid & 7) * 4 + ((bid >> 3) & 3);
  const int j = bid >> 5;
  const int b = j >> 1, half = j & 1;
  const int tid = threadIdx.x, lane = tid & 63;
  const int wave = __builtin_amdgcn_readfirstlane(tid >> 6);
  const int r = lane >> 4, c = lane & 15;

  Ctx cx;
  cx.kA = mk(p.kA, KVBYTES); cx.vA = mk(p.vA, KVBYTES); cx.kB = mk(p.kB, KVBYTES); cx.vB = mk(p.vB, KVBYTES);
  const unsigned bh = (unsigned)(b * NH + h) * SK;
  cx.offB = (bh + (SK - WIN) + half * 512) * (DH * 2) + wave * 4096;
  cx.offA = (bh + half * 2048) * (DH * 2) + wave * 4096;
  cx.vo = lane * 16;
  const rsrc_t rqA = mk(p.wqA, WBYTES), rqB = mk(p.wqB, WBYTES), roA = mk(p.woA, WBYTES), roB = mk(p.woB, WBYTES);
  const unsigned qvo = ((64 * wave + 16 * r) * HD + h * DH + 8 * c) * 2;
  const unsigned ovo = ((h * DH + (lane >> 3)) * DM + 64 * wave + 8 * (lane & 7)) * 2;

  u32x4 WA[NL], WB[NL], K0[NL], ring[R], xv[2];
  {
    const char* xp = reinterpret_cast<const char*>(p.x) + (b * DM + 64 * wave + 16 * r) * 2;
    xv[0] = *reinterpret_cast<const u32x4*>(xp);
    xv[1] = *reinterpret_cast<const u32x4*>(xp + 16);
  }
  if (tid == 0) reinterpret_cast<unsigned*>(lds)[L_CNT] = 0u;
  Pro pro{cx, WA, WB, K0, rqA, rqB, qvo};
  PIN();
  pro.range<0, 2 * PWIN>();
  PIN();
  STAMP(1);

  float* yb = p.y + (size_t)((b * NH + h) * 4 + half) * DM;
  float* mlb = p.ml + (size_t)((b * NH + h) * 4 + half) * 2;

  float qa[8];
#pragma unroll
  for (int k = 0; k < 8; ++k) qa[k] = 0.f;
  qb_loop<0>(pro, qa, xv);
  q_post(qa, lds + L_QP, wave, r, c);
  __syncthreads();
  STAMP(2);
  u32x4 q = q_gather(lds + L_QP, c);
  PIN();
  pro.range<2 * (NL + PWIN), 64>();
  PIN();

  State st;
  float x[NL], pr;
  reset(st);
#pragma unroll
  for (int k = 0; k < 8; ++k) qa[k] = 0.f;
#pragma unroll
  for (int i = 0; i < NL; ++i) x[i] = qk(K0[i], q);
  pr = softmax_step(st, x, c);
  PIN();
  STAMP(3);
  v0_loop<0>(cx, pro, ring, st, pr, qa, xv, lds, wave, r, c, lane);
  STAMP(4);
  wave_record(st, lds + L_REC + NW * REC, wave, r, c, lane);
  reset(st);
  while (__hip_atomic_load(reinterpret_cast<unsigned*>(lds) + L_CNT, __ATOMIC_RELAXED, __HIP_MEMORY_SCOPE_WORKGROUP) < (unsigned)NW)
    __builtin_amdgcn_s_sleep(1);
  __builtin_amdgcn_fence(__ATOMIC_ACQUIRE, "workgroup");
  q = q_gather(lds + L_QP + NW * 128, c);
  PIN();
  STAMP(5);

  KPhase<1, 0, 0>::run(cx, ring, q, x, WB, roB, ovo);
  pr = softmax_step(st, x, c);
  PIN();
  VPhase<1, 0, 8>::run(cx, ring, st, pr, WB, roB, ovo);
  STAMP(6);
  STAMP(7);
  KPhase<2, 0, 0>::run(cx, ring, q, x, WA, roA, ovo);
  pr = softmax_step(st, x, c);
  PIN();
  VPhase<2, 0, 8>::run(cx, ring, st, pr, WA, roA, ovo);
  STAMP(8);
  KPhase<3, 0, -1>::run(cx, ring, q, x, WA, roA, ovo);
  pr = softmax_step(st, x, c);
  wo_pair(WB);
  PIN();
  VPhase<3, 0, -1>::run(cx, ring, st, pr, WA, roA, ovo);
  wo_pair(WA);
  STAMP(9);
  KPhase<4, 0, -1>::run(cx, ring, q, x, WA, roA, ovo);
  pr = softmax_step(st, x, c);
  PIN();
  VPhase<4, 0, -1>::run(cx, ring, st, pr, WA, roA, ovo);
  STAMP(10);

  wave_record(st, lds + L_REC, wave, r, c, lane);
  __syncthreads();
  STAMP(11);
  wg_merge(lds, tid, mlb);
  __syncthreads();
  STAMP(12);
  out_proj(WA, lds + L_O, wave, lane, yb);
  out_proj(WB, lds + L_O + 64, wave, lane, yb + 2 * DM);
  STAMP(13);
  STAMP_FLUSH(0, 14);
}

__global__ void __launch_bounds__(256) combine(Params p) {
  __shared__ __attribute__((aligned(16))) float red[4][32];
  __shared__ float gpart[4];
  STAMP_DECL;
  STAMP(14);
  const int b = blockIdx.x >> 4, cc = blockIdx.x & 15;
  const int tid = threadIdx.x, lane = tid & 63, wave = tid >> 6;
  const int h = tid >> 3, c4 = tid & 7;
  const float* ml = p.ml + (size_t)(b * NH + h) * 8;
  const f32x4 mlA = *reinterpret_cast<const f32x4*>(ml);
  const f32x4 mlB = *reinterpret_cast<const f32x4*>(ml + 4);
  const float* y = p.y + (size_t)(b * NH + h) * 4 * DM + cc * 32 + c4 * 4;
  const f32x4 y0 = *reinterpret_cast<const f32x4*>(y);
  const f32x4 y1 = *reinterpret_cast<const f32x4*>(y + DM);
  const f32x4 y2 = *reinterpret_cast<const f32x4*>(y + 2 * DM);
  const f32x4 y3 = *reinterpret_cast<const f32x4*>(y + 3 * DM);
  const unsigned xw = reinterpret_cast<const unsigned*>(p.x)[b * (DM / 2) + tid];
  const unsigned gw = reinterpret_cast<const unsigned*>(p.gate)[tid];
  float gs = bf_lo(xw) * bf_lo(gw) + bf_hi(xw) * bf_hi(gw);
  gs = xrow_sum(row_sum(gs));
  if (lane == 0) gpart[wave] = gs;

  const float MA = fmaxf(mlA[0], mlA[2]), MB = fmaxf(mlB[0], mlB[2]);
  const float a0 = __builtin_amdgcn_exp2f(mlA[0] - MA), a1 = __builtin_amdgcn_exp2f(mlA[2] - MA);
  const float b0 = __builtin_amdgcn_exp2f(mlB[0] - MB), b1 = __builtin_amdgcn_exp2f(mlB[2] - MB);
  const float iA = 1.f / (a0 * mlA[1] + a1 * mlA[3]);
  const float iB = 1.f / (b0 * mlB[1] + b1 * mlB[3]);
  f32x4 sa = (y0 * a0 + y1 * a1) * iA;
  f32x4 sb = (y2 * b0 + y3 * b1) * iB;
  __syncthreads();
  const float z = gpart[0] + gpart[1] + gpart[2] + gpart[3];
  const float g = 1.f / (1.f + __expf(-z));
  f32x4 s = sa * g + sb * (1.f - g);
#pragma unroll
  for (int k = 0; k < 4; ++k) { float t = s[k]; t += dpp<XOR8>(t); s[k] = xrow_sum(t); }
  if (lane < 8) *reinterpret_cast<f32x4*>(&red[wave][4 * lane]) = s;
  __syncthreads();
  if (tid < 32) {
    const float v = red[0][tid] + red[1][tid] + red[2][tid] + red[3][tid];
    p.out[b * DM + cc * 32 + tid] = __float2bfloat16(v);
  }
  STAMP(15);
  STAMP_FLUSH(14, 16);
}

}

extern "C" void kernel_launch(void* const* d_in, const int* in_sizes, int n_in, void* d_out, int out_size, void* d_ws,
                              size_t ws_size, hipStream_t stream) {
  Params p;
  p.x = (const uint16_t*)d_in[0];
  p.kA = (const uint16_t*)d_in[1];
  p.vA = (const uint16_t*)d_in[2];
  p.wqA = (const uint16_t*)d_in[3];
  p.woA = (const uint16_t*)d_in[4];
  p.kB = (const uint16_t*)d_in[6];
  p.vB = (const uint16_t*)d_in[7];
  p.wqB = (const uint16_t*)d_in[8];
  p.woB = (const uint16_t*)d_in[9];
  p.gate = (const uint16_t*)d_in[10];
  p.y = (float*)d_ws;
  p.ml = (float*)((char*)d_ws + (size_t)NB * NH * 4 * DM * 4);
  p.out = (__hip_bfloat16*)d_out;
  attn_partial<<<256, NT, 0, stream>>>(p);
  combine<<<64, 256, 0, stream>>>(p);
}
```

```cpp
#define WBAUX 0
#include <hip/hip_runtime.h>
#include <hip/hip_bf16.h>
#include <stdint.h>

#define RING 4
#define WAUX 2
#define KBAUX 2
#define VBAUX 2
#define AAUX 2
#define PWIN 4

#define STAMP(k)
#define STAMP_DECL
#define STAMP_FLUSH(a, b)

namespace {

constexpr int NB = 4, DM = 512, NH = 32, SK = 4096, DH = 128, WIN = 1024;
constexpr int HD = NH * DH;

using u32x4 = __attribute__((ext_vector_type(4))) unsigned;
using f32x4 = __attribute__((ext_vector_type(4))) float;
using f32x2 = __attribute__((ext_vector_type(2))) float;
using bf16x2 = __attribute__((ext_vector_type(2))) __bf16;
using rsrc_t = __amdgpu_buffer_rsrc_t;

__device__ __forceinline__ float dot2(unsigned k, unsigned q, float acc) {
  return __builtin_amdgcn_fdot2_f32_bf16(__builtin_bit_cast(bf16x2, k), __builtin_bit_cast(bf16x2, q), acc, false);
}
__device__ __forceinline__ unsigned cvtpk(float lo, float hi) {
  unsigned r;
  asm volatile("v_cvt_pk_bf16_f32 %0, %1, %2" : "=v"(r) : "v"(lo), "v"(hi));
  return r;
}
template <int CTRL>
__device__ __forceinline__ float dpp(float x) {
  return __builtin_bit_cast(float, __builtin_amdgcn_mov_dpp(__builtin_bit_cast(int, x), CTRL, 0xf, 0xf, true));
}
constexpr int XOR1 = 0xB1, XOR2 = 0x4E, XOR7 = 0x141, XOR8 = 0x128;
constexpr int BCAST = 0x150;

__device__ __forceinline__ float bf_lo(unsigned w) { return __builtin_bit_cast(float, w << 16); }
__device__ __forceinline__ float bf_hi(unsigned w) { return __builtin_bit_cast(float, w & 0xffff0000u); }
__device__ __forceinline__ float rbf(float v) { return bf_lo(cvtpk(v, v)); }

__device__ __forceinline__ float tr32(float u, float w) {
  auto t = __builtin_amdgcn_permlane32_swap(__float_as_uint(u), __float_as_uint(w), false, false);
  return __uint_as_float(t[0]) + __uint_as_float(t[1]);
}
__device__ __forceinline__ float tr16(float u, float w) {
  auto t = __builtin_amdgcn_permlane16_swap(__float_as_uint(u), __float_as_uint(w), false, false);
  return __uint_as_float(t[0]) + __uint_as_float(t[1]);
}
__device__ __forceinline__ float xrow_sum(float x) { x = tr16(x, x); return tr32(x, x); }
__device__ __forceinline__ float xrow_max(float x) {
  auto s = __builtin_amdgcn_permlane16_swap(__float_as_uint(x), __float_as_uint(x), false, false);
  x = fmaxf(__uint_as_float(s[0]), __uint_as_float(s[1]));
  auto t = __builtin_amdgcn_permlane32_swap(__float_as_uint(x), __float_as_uint(x), false, false);
  return fmaxf(__uint_as_float(t[0]), __uint_as_float(t[1]));
}
__device__ __forceinline__ float row_max(float x) {
  x = fmaxf(x, dpp<XOR8>(x));
  x = fmaxf(x, dpp<XOR7>(x));
  x = fmaxf(x, dpp<XOR2>(x));
  x = fmaxf(x, dpp<XOR1>(x));
  return x;
}
__device__ __forceinline__ float row_sum(float x) {
  x += dpp<XOR8>(x);
  x += dpp<XOR7>(x);
  x += dpp<XOR2>(x);
  x += dpp<XOR1>(x);
  return x;
}
__device__ __forceinline__ f32x2 rows_reduce8(const float (&a)[8]) {
  const float u0 = tr32(a[0], a[4]), u1 = tr32(a[1], a[5]), u2 = tr32(a[2], a[6]), u3 = tr32(a[3], a[7]);
  return f32x2{tr16(u0, u2), tr16(u1, u3)};
}
#define PIN() __builtin_amdgcn_sched_barrier(0)

constexpr int NW = 8;
constexpr int NT = NW * 64;
constexpr int NL = 16;
constexpr int R = RING;
constexpr int NSEG = 5;
constexpr int LTOT = NSEG * 32;
constexpr float SCALE = 0.08838834764831845f, LOG2E = 1.4426950408889634f;
constexpr unsigned KVBYTES = (unsigned)NB * NH * SK * DH * 2;
constexpr unsigned WBYTES = (unsigned)DM * HD * 2;

struct Params {
  const uint16_t* x;
  const uint16_t* kA; const uint16_t* vA; const uint16_t* wqA; const uint16_t* woA;
  const uint16_t* kB; const uint16_t* vB; const uint16_t* wqB; const uint16_t* woB;
  const uint16_t* gate;
  float* y;
  float* ml;
  __hip_bfloat16* out;
};

constexpr int L_QP = 0;
constexpr int REC = 132;
constexpr int L_REC = L_QP + 2 * NW * 128;
constexpr int L_O = L_REC + 2 * NW * REC;
constexpr int L_CNT = L_O + 2 * 64;
constexpr int L_END = L_CNT + 4;

struct Ctx {
  rsrc_t kA, vA, kB, vB;
  unsigned offA, offB;
  unsigned vo;
};

template <int L>
__device__ __forceinline__ u32x4 ld_stream(const Ctx& c) {
  constexpr int S = L / 32, PH = (L / 16) & 1, N = L % 16;
  constexpr unsigned rel = (N / 4) * 32768u + (S > 0 ? (S - 1) * 131072u : 0u);
  const unsigned so = (S == 0 ? c.offB : c.offA) + rel;
  const rsrc_t& r = S == 0 ? (PH ? c.vB : c.kB) : (PH ? c.vA : c.kA);
  return __builtin_amdgcn_raw_buffer_load_b128(r, c.vo + (N % 4) * 1024, so, S == 0 ? (PH ? VBAUX : KBAUX) : AAUX);
}
template <bool ISB>
__device__ __forceinline__ u32x4 ld_w(const rsrc_t& r, unsigned vo, unsigned so) {
  return __builtin_amdgcn_raw_buffer_load_b128(r, vo, so, ISB ? WBAUX : WAUX);
}

template <int I>
__device__ __forceinline__ void q_step(float (&qa)[8], const u32x4& w, const u32x4 (&xv)[2]) {
  const unsigned xw = xv[I >> 3][(I >> 1) & 3];
  const float xf = (I & 1) ? bf_hi(xw) : bf_lo(xw);
#pragma unroll
  for (int k = 0; k < 4; ++k) {
    qa[2 * k] = fmaf(xf, bf_lo(w[k]), qa[2 * k]);
    qa[2 * k + 1] = fmaf(xf, bf_hi(w[k]), qa[2 * k + 1]);
  }
}
__device__ __forceinline__ void q_post(const float (&qa)[8], float* lds, int wave, int r, int c) {
  *reinterpret_cast<f32x2*>(lds + wave * 128 + 8 * c + 2 * r) = rows_reduce8(qa);
}
__device__ __forceinline__ u32x4 q_gather(const float* lds, int c) {
  f32x4 a = {0.f, 0.f, 0.f, 0.f}, b = {0.f, 0.f, 0.f, 0.f};
#pragma unroll
  for (int w = 0; w < NW; ++w) {
    a += *reinterpret_cast<const f32x4*>(lds + w * 128 + 8 * c);
    b += *reinterpret_cast<const f32x4*>(lds + w * 128 + 8 * c + 4);
  }
  return u32x4{cvtpk(a[0], a[1]), cvtpk(a[2], a[3]), cvtpk(b[0], b[1]), cvtpk(b[2], b[3])};
}

struct State { float m, l, acc[8]; };

__device__ __forceinline__ float qk(const u32x4& k, const u32x4& q) {
  float d = dot2(k[0], q[0], 0.f);
  d = dot2(k[1], q[1], d);
  d = dot2(k[2], q[2], d);
  return dot2(k[3], q[3], d);
}

__device__ __forceinline__ float softmax_step(State& st, float (&x)[NL], int c) {
#define TR_STEP(HALF, CTRL, BIT)                                          \
  _Pragma("unroll") for (int h = 0; h < (HALF); ++h) {                     \
    const float lo_ = x[h], hi_ = x[h + (HALF)];                           \
    const float t1_ = lo_ + dpp<CTRL>(lo_), t2_ = hi_ + dpp<CTRL>(hi_);    \
    x[h] = (c & (BIT)) ? t2_ : t1_;                                        \
  }
  TR_STEP(8, XOR8, 8) TR_STEP(4, XOR7, 4) TR_STEP(2, XOR2, 2) TR_STEP(1, XOR1, 1)
#undef TR_STEP
  const float s = rbf(rbf(x[0]) * SCALE) * LOG2E;
  const float mn = fmaxf(st.m, row_max(s));
  const float alpha = __builtin_amdgcn_exp2f(st.m - mn);
  const float p = __builtin_amdgcn_exp2f(s - mn);
  st.l = fmaf(st.l, alpha, p);
  st.m = mn;
#pragma unroll
  for (int k = 0; k < 8; ++k) st.acc[k] *= alpha;
  return p;
}

template <int I>
__device__ __forceinline__ void pv_step(State& st, float p, const u32x4& v) {
  const float pb = dpp<BCAST + I>(p);
#pragma unroll
  for (int k = 0; k < 4; ++k) {
    st.acc[2 * k] = fmaf(pb, bf_lo(v[k]), st.acc[2 * k]);
    st.acc[2 * k + 1] = fmaf(pb, bf_hi(v[k]), st.acc[2 * k + 1]);
  }
}

template <int S, int N, int TR0>
struct KPhase {
  static __device__ __forceinline__ void run(const Ctx& c, u32x4 (&ring)[R], const u32x4& q, float (&x)[NL], u32x4 (&W)[NL],
                                             const rsrc_t& rw, unsigned wvo) {
    constexpr int L = S * 32 + N;
    x[N] = qk(ring[L % R], q);
    PIN();
    ring[L % R] = ld_stream<L + R>(c);
    if constexpr (TR0 >= 0 && (N & 1)) W[TR0 + N / 2] = ld_w<S == 1>(rw, wvo, (TR0 + N / 2) * 8 * DM * 2);
    PIN();
    KPhase<S, N + 1, TR0>::run(c, ring, q, x, W, rw, wvo);
  }
};
template <int S, int TR0>
struct KPhase<S, NL, TR0> {
  static __device__ __forceinline__ void run(const Ctx&, u32x4 (&)[R], const u32x4&, float (&)[NL], u32x4 (&)[NL], const rsrc_t&, unsigned) {}
};
template <int S, int N, int TR0>
struct VPhase {
  static __device__ __forceinline__ void run(const Ctx& c, u32x4 (&ring)[R], State& st, float p, u32x4 (&W)[NL], const rsrc_t& rw,
                                             unsigned wvo) {
    constexpr int L = S * 32 + 16 + N;
    pv_step<N>(st, p, ring[L % R]);
    PIN();
    if constexpr (L + R < LTOT) ring[L % R] = ld_stream<L + R>(c);
    if constexpr (TR0 >= 0 && (N & 1)) W[TR0 + N / 2] = ld_w<S == 1>(rw, wvo, (TR0 + N / 2) * 8 * DM * 2);
    PIN();
    VPhase<S, N + 1, TR0>::run(c, ring, st, p, W, rw, wvo);
  }
};
template <int S, int TR0>
struct VPhase<S, NL, TR0> {
  static __device__ __forceinline__ void run(const Ctx&, u32x4 (&)[R], State&, float, u32x4 (&)[NL], const rsrc_t&, unsigned) {}
};

__device__ __forceinline__ void wave_record(State& st, float* lds, int wave, int r, int c, int lane) {
  const float M = xrow_max(st.m);
  const float w = __builtin_amdgcn_exp2f(st.m - M);
  const float L = xrow_sum(row_sum(st.l) * w);
  float a[8];
#pragma unroll
  for (int k = 0; k < 8; ++k) a[k] = st.acc[k] * w;
  float* rec = lds + wave * REC;
  *reinterpret_cast<f32x2*>(rec + 8 * c + 2 * r) = rows_reduce8(a);
  if (lane == 0) *reinterpret_cast<f32x2*>(rec + 128) = f32x2{M, L};
}
__device__ __forceinline__ void wg_merge(float* lds, int tid, float* ml_out) {
  if (tid < 128) {
    const int br = tid >> 6, d = ((tid >> 3) & 7) + 16 * (tid & 7);
    const float* rec = lds + L_REC + br * NW * REC;
    float M = rec[128];
#pragma unroll
    for (int w = 1; w < NW; ++w) M = fmaxf(M, rec[w * REC + 128]);
    float o0 = 0.f, o1 = 0.f, L = 0.f;
#pragma unroll
    for (int w = 0; w < NW; ++w) {
      const float wt = __builtin_amdgcn_exp2f(rec[w * REC + 128] - M);
      o0 = fmaf(wt, rec[w * REC + d], o0);
      o1 = fmaf(wt, rec[w * REC + d + 8], o1);
      L = fmaf(wt, rec[w * REC + 129], L);
    }
    reinterpret_cast<unsigned*>(lds)[L_O + tid] = cvtpk(o0, o1);
    if ((tid & 63) == 0) *reinterpret_cast<float2*>(ml_out + 4 * br) = make_float2(M, L);
  }
}
__device__ __forceinline__ void wo_pair(u32x4 (&W)[NL]) {
#pragma unroll
  for (int j = 0; j < NL / 2; ++j)
#pragma unroll
    for (int k = 0; k < 4; ++k) {
      const unsigned a = W[2 * j][k], b = W[2 * j + 1][k];
      W[2 * j][k] = __builtin_amdgcn_perm(b, a, 0x05040100u);
      W[2 * j + 1][k] = __builtin_amdgcn_perm(b, a, 0x07060302u);
    }
}
__device__ __forceinline__ void out_proj(const u32x4 (&W)[NL], const float* lds, int wave, int lane, float* y_out) {
  const int r8 = lane >> 3, c8 = lane & 7;
  const u32x4 oa = *reinterpret_cast<const u32x4*>(lds + r8 * 8), ob = *reinterpret_cast<const u32x4*>(lds + r8 * 8 + 4);
  float y[8];
#pragma unroll
  for (int k = 0; k < 8; ++k) y[k] = 0.f;
#pragma unroll
  for (int j = 0; j < NL / 2; ++j) {
    const unsigned o = j < 4 ? oa[j & 3] : ob[j & 3];
#pragma unroll
    for (int k = 0; k < 4; ++k) {
      y[2 * k] = dot2(W[2 * j][k], o, y[2 * k]);
      y[2 * k + 1] = dot2(W[2 * j + 1][k], o, y[2 * k + 1]);
    }
  }
  const f32x2 t = rows_reduce8(y);
  const float t0 = t[0] + dpp<XOR8>(t[0]), t1 = t[1] + dpp<XOR8>(t[1]);
  y_out[64 * wave + 8 * c8 + r8] = (lane & 8) ? t1 : t0;
}

__device__ __forceinline__ rsrc_t mk(const void* p, unsigned bytes) {
  return __builtin_amdgcn_make_buffer_rsrc(const_cast<void*>(p), 0, bytes, 0x00020000);
}

struct Pro {
  const Ctx& c; u32x4 (&WA)[NL]; u32x4 (&WB)[NL]; u32x4 (&K0)[NL];
  const rsrc_t& rqA; const rsrc_t& rqB; unsigned qvo;
  template <int P>
  __device__ __forceinline__ void issue() {
    if constexpr (P < 32) {
      if constexpr ((P & 1) == 0) WB[P / 2] = ld_w<true>(rqB, qvo, (P / 2) * HD * 2);
      else K0[P / 2] = ld_stream<P / 2>(c);
    } else if constexpr (P < 56) {
      constexpr int T = (P - 32) / 3, U = (P - 32) % 3;
      if constexpr (U < 2) WA[2 * T + U] = ld_w<false>(rqA, qvo, (2 * T + U) * HD * 2);
      else WB[T] = ld_stream<16 + T>(c);
    } else if constexpr (P < 64) {
      WB[P - 48] = ld_stream<16 + P - 48>(c);
    }
  }
  template <int P0, int P1>
  __device__ __forceinline__ void range() {
    if constexpr (P0 < P1) { issue<P0>(); range<P0 + 1, P1>(); }
  }
};
template <int I>
__device__ __forceinline__ void qb_loop(Pro& pro, float (&qa)[8], const u32x4 (&xv)[2]) {
  if constexpr (I < NL) {
    q_step<I>(qa, pro.WB[I], xv);
    PIN();
    pro.template range<2 * (I + PWIN), 2 * (I + PWIN) + 2>();
    PIN();
    qb_loop<I + 1>(pro, qa, xv);
  }
}
template <int N>
__device__ __forceinline__ void v0_loop(const Ctx& c, Pro& pro, u32x4 (&ring)[R], State& st, float p, float (&qa)[8],
                                        const u32x4 (&xv)[2], float* lds, int wave, int r, int cc, int lane) {
  if constexpr (N < NL) {
    if constexpr (N < 8) { q_step<2 * N>(qa, pro.WA[2 * N], xv); q_step<2 * N + 1>(qa, pro.WA[2 * N + 1], xv); }
    pv_step<N>(st, p, pro.WB[N]);
    PIN();
    if constexpr (N == 7) {
      q_post(qa, lds + L_QP + NW * 128, wave, r, cc);
      __builtin_amdgcn_fence(__ATOMIC_RELEASE, "workgroup");
      if (lane == 0) __hip_atomic_fetch_add(reinterpret_cast<unsigned*>(lds) + L_CNT, 1u, __ATOMIC_RELAXED, __HIP_MEMORY_SCOPE_WORKGROUP);
    }
    if constexpr (N >= NL - R) ring[(32 + N - (NL - R)) % R] = ld_stream<32 + N - (NL - R)>(c);
    PIN();
    v0_loop<N + 1>(c, pro, ring, st, p, qa, xv, lds, wave, r, cc, lane);
  }
}

__device__ __forceinline__ void reset(State& st) {
  st.m = -INFINITY; st.l = 0.f;
#pragma unroll
  for (int k = 0; k < 8; ++k) st.acc[k] = 0.f;
}

__global__ void __launch_bounds__(NT, 2) attn_partial(Params p) {
  __shared__ __attribute__((aligned(16))) float lds[L_END];
  const int bid = blockIdx.x;
  STAMP_DECL;
  STAMP(0);
  const int h = (bid & 7) * 4 + ((bid >> 3) & 3);
  const int j = bid >> 5;
  const int b = j >> 1, half = j & 1;
  const int tid = threadIdx.x, lane = tid & 63;
  const int wave = __builtin_amdgcn_readfirstlane(tid >> 6);
  const int r = lane >> 4, c = lane & 15;

  Ctx cx;
  cx.kA = mk(p.kA, KVBYTES); cx.vA = mk(p.vA, KVBYTES); cx.kB = mk(p.kB, KVBYTES); cx.vB = mk(p.vB, KVBYTES);
  const unsigned bh = (unsigned)(b * NH + h) * SK;
  cx.offB = (bh + (SK - WIN) + half * 512) * (DH * 2) + wave * 4096;
  cx.offA = (bh + half * 2048) * (DH * 2) + wave * 4096;
  cx.vo = lane * 16;
  const rsrc_t rqA = mk(p.wqA, WBYTES), rqB = mk(p.wqB, WBYTES), roA = mk(p.woA, WBYTES), roB = mk(p.woB, WBYTES);
  const unsigned qvo = ((64 * wave + 16 * r) * HD + h * DH + 8 * c) * 2;
  const unsigned ovo = ((h * DH + (lane >> 3)) * DM + 64 * wave + 8 * (lane & 7)) * 2;

  u32x4 WA[NL], WB[NL], K0[NL], ring[R], xv[2];
  {
    const char* xp = reinterpret_cast<const char*>(p.x) + (b * DM + 64 * wave + 16 * r) * 2;
    xv[0] = *reinterpret_cast<const u32x4*>(xp);
    xv[1] = *reinterpret_cast<const u32x4*>(xp + 16);
  }
  if (tid == 0) reinterpret_cast<unsigned*>(lds)[L_CNT] = 0u;
  Pro pro{cx, WA, WB, K0, rqA, rqB, qvo};
  PIN();
  pro.range<0, 2 * PWIN>();
  PIN();
  STAMP(1);

  float* yb = p.y + (size_t)((b * NH + h) * 4 + half) * DM;
  float* mlb = p.ml + (size_t)((b * NH + h) * 4 + half) * 2;

  float qa[8];
#pragma unroll
  for (int k = 0; k < 8; ++k) qa[k] = 0.f;
  qb_loop<0>(pro, qa, xv);
  q_post(qa, lds + L_QP, wave, r, c);
  __syncthreads();
  STAMP(2);
  u32x4 q = q_gather(lds + L_QP, c);
  PIN();
  pro.range<2 * (NL + PWIN), 64>();
  PIN();

  State st;
  float x[NL], pr;
  reset(st);
#pragma unroll
  for (int k = 0; k < 8; ++k) qa[k] = 0.f;
#pragma unroll
  for (int i = 0; i < NL; ++i) x[i] = qk(K0[i], q);
  pr = softmax_step(st, x, c);
  PIN();
  STAMP(3);
  v0_loop<0>(cx, pro, ring, st, pr, qa, xv, lds, wave, r, c, lane);
  STAMP(4);
  wave_record(st, lds + L_REC + NW * REC, wave, r, c, lane);
  reset(st);
  while (__hip_atomic_load(reinterpret_cast<unsigned*>(lds) + L_CNT, __ATOMIC_RELAXED, __HIP_MEMORY_SCOPE_WORKGROUP) < (unsigned)NW)
    __builtin_amdgcn_s_sleep(1);
  __builtin_amdgcn_fence(__ATOMIC_ACQUIRE, "workgroup");
  q = q_gather(lds + L_QP + NW * 128, c);
  PIN();
  STAMP(5);

  KPhase<1, 0, 0>::run(cx, ring, q, x, WB, roB, ovo);
  pr = softmax_step(st, x, c);
  PIN();
  VPhase<1, 0, 8>::run(cx, ring, st, pr, WB, roB, ovo);
  STAMP(6);
  STAMP(7);
  KPhase<2, 0, 0>::run(cx, ring, q, x, WA, roA, ovo);
  pr = softmax_step(st, x, c);
  PIN();
  VPhase<2, 0, 8>::run(cx, ring, st, pr, WA, roA, ovo);
  STAMP(8);
  KPhase<3, 0, -1>::run(cx, ring, q, x, WA, roA, ovo);
  pr = softmax_step(st, x, c);
  wo_pair(WB);
  PIN();
  VPhase<3, 0, -1>::run(cx, ring, st, pr, WA, roA, ovo);
  wo_pair(WA);
  STAMP(9);
  KPhase<4, 0, -1>::run(cx, ring, q, x, WA, roA, ovo);
  pr = softmax_step(st, x, c);
  PIN();
  VPhase<4, 0, -1>::run(cx, ring, st, pr, WA, roA, ovo);
  STAMP(10);

  wave_record(st, lds + L_REC, wave, r, c, lane);
  __syncthreads();
  STAMP(11);
  wg_merge(lds, tid, mlb);
  __syncthreads();
  STAMP(12);
  out_proj(WA, lds + L_O, wave, lane, yb);
  out_proj(WB, lds + L_O + 64, wave, lane, yb + 2 * DM);
  STAMP(13);
  STAMP_FLUSH(0, 14);
}

__global__ void __launch_bounds__(256) combine(Params p) {
  __shared__ __attribute__((aligned(16))) float red[4][32];
  __shared__ float gpart[4];
  STAMP_DECL;
  STAMP(14);
  const int b = blockIdx.x >> 4, cc = blockIdx.x & 15;
  const int tid = threadIdx.x, lane = tid & 63, wave = tid >> 6;
  const int h = tid >> 3, c4 = tid & 7;
  const float* ml = p.ml + (size_t)(b * NH + h) * 8;
  const f32x4 mlA = *reinterpret_cast<const f32x4*>(ml);
  const f32x4 mlB = *reinterpret_cast<const f32x4*>(ml + 4);
  const float* y = p.y + (size_t)(b * NH + h) * 4 * DM + cc * 32 + c4 * 4;
  const f32x4 y0 = *reinterpret_cast<const f32x4*>(y);
  const f32x4 y1 = *reinterpret_cast<const f32x4*>(y + DM);
  const f32x4 y2 = *reinterpret_cast<const f32x4*>(y + 2 * DM);
  const f32x4 y3 = *reinterpret_cast<const f32x4*>(y + 3 * DM);
  const unsigned xw = reinterpret_cast<const unsigned*>(p.x)[b * (DM / 2) + tid];
  const unsigned gw = reinterpret_cast<const unsigned*>(p.gate)[tid];
  float gs = bf_lo(xw) * bf_lo(gw) + bf_hi(xw) * bf_hi(gw);
  gs = xrow_sum(row_sum(gs));
  if (lane == 0) gpart[wave] = gs;

  const float MA = fmaxf(mlA[0], mlA[2]), MB = fmaxf(mlB[0], mlB[2]);
  const float a0 = __builtin_amdgcn_exp2f(mlA[0] - MA), a1 = __builtin_amdgcn_exp2f(mlA[2] - MA);
  const float b0 = __builtin_amdgcn_exp2f(mlB[0] - MB), b1 = __builtin_amdgcn_exp2f(mlB[2] - MB);
  const float iA = 1.f / (a0 * mlA[1] + a1 * mlA[3]);
  const float iB = 1.f / (b0 * mlB[1] + b1 * mlB[3]);
  f32x4 sa = (y0 * a0 + y1 * a1) * iA;
  f32x4 sb = (y2 * b0 + y3 * b1) * iB;
  __syncthreads();
  const float z = gpart[0] + gpart[1] + gpart[2] + gpart[3];
  const float g = 1.f / (1.f + __expf(-z));
  f32x4 s = sa * g + sb * (1.f - g);
#pragma unroll
  for (int k = 0; k < 4; ++k) { float t = s[k]; t += dpp<XOR8>(t); s[k] = xrow_sum(t); }
  if (lane < 8) *reinterpret_cast<f32x4*>(&red[wave][4 * lane]) = s;
  __syncthreads();
  if (tid < 32) {
    const float v = red[0][tid] + red[1][tid] + red[2][tid] + red[3][tid];
    p.out[b * DM + cc * 32 + tid] = __float2bfloat16(v);
  }
  STAMP(15);
  STAMP_FLUSH(14, 16);
}

}

extern "C" void kernel_launch(void* const* d_in, const int* in_sizes, int n_in, void* d_out, int out_size, void* d_ws,
                              size_t ws_size, hipStream_t stream) {
  Params p;
  p.x = (const uint16_t*)d_in[0];
  p.kA = (const uint16_t*)d_in[1];
  p.vA = (const uint16_t*)d_in[2];
  p.wqA = (const uint16_t*)d_in[3];
  p.woA = (const uint16_t*)d_in[4];
  p.kB = (const uint16_t*)d_in[6];
  p.vB = (const uint16_t*)d_in[7];
  p.wqB = (const uint16_t*)d_in[8];
  p.woB = (const uint16_t*)d_in[9];
  p.gate = (const uint16_t*)d_in[10];
  p.y = (float*)d_ws;
  p.ml = (float*)((char*)d_ws + (size_t)NB * NH * 4 * DM * 4);
  p.out = (__hip_bfloat16*)d_out;
  attn_partial<<<256, NT, 0, stream>>>(p);
  combine<<<64, 256, 0, stream>>>(p);
}
```

```cpp
#include <hip/hip_runtime.h>
#include <hip/hip_bf16.h>
#include <stdint.h>
#include <stdio.h>

#define RING 4
#define WAUX 2
#define WBAUX WAUX
#define KBAUX 2
#define VBAUX 2
#define AAUX 2
#define PWIN 4

#define STAMP(k)
#define STAMP_DECL
#define STAMP_FLUSH(a, b)

namespace {

constexpr int NB = 4, DM = 512, NH = 32, SK = 4096, DH = 128, WIN = 1024;
constexpr int HD = NH * DH;

using u32x4 = __attribute__((ext_vector_type(4))) unsigned;
using f32x4 = __attribute__((ext_vector_type(4))) float;
using f32x2 = __attribute__((ext_vector_type(2))) float;
using bf16x2 = __attribute__((ext_vector_type(2))) __bf16;
using rsrc_t = __amdgpu_buffer_rsrc_t;

__device__ __forceinline__ float dot2(unsigned k, unsigned q, float acc) {
  return __builtin_amdgcn_fdot2_f32_bf16(__builtin_bit_cast(bf16x2, k), __builtin_bit_cast(bf16x2, q), acc, false);
}
__device__ __forceinline__ unsigned cvtpk(float lo, float hi) {
  unsigned r;
  asm volatile("v_cvt_pk_bf16_f32 %0, %1, %2" : "=v"(r) : "v"(lo), "v"(hi));
  return r;
}
template <int CTRL>
__device__ __forceinline__ float dpp(float x) {
  return __builtin_bit_cast(float, __builtin_amdgcn_mov_dpp(__builtin_bit_cast(int, x), CTRL, 0xf, 0xf, true));
}
constexpr int XOR1 = 0xB1, XOR2 = 0x4E, XOR7 = 0x141, XOR8 = 0x128;
constexpr int BCAST = 0x150;

__device__ __forceinline__ float bf_lo(unsigned w) { return __builtin_bit_cast(float, w << 16); }
__device__ __forceinline__ float bf_hi(unsigned w) { return __builtin_bit_cast(float, w & 0xffff0000u); }
__device__ __forceinline__ float rbf(float v) { return bf_lo(cvtpk(v, v)); }

__device__ __forceinline__ float tr32(float u, float w) {
  auto t = __builtin_amdgcn_permlane32_swap(__float_as_uint(u), __float_as_uint(w), false, false);
  return __uint_as_float(t[0]) + __uint_as_float(t[1]);
}
__device__ __forceinline__ float tr16(float u, float w) {
  auto t = __builtin_amdgcn_permlane16_swap(__float_as_uint(u), __float_as_uint(w), false, false);
  return __uint_as_float(t[0]) + __uint_as_float(t[1]);
}
__device__ __forceinline__ float xrow_sum(float x) { x = tr16(x, x); return tr32(x, x); }
__device__ __forceinline__ float xrow_max(float x) {
  auto s = __builtin_amdgcn_permlane16_swap(__float_as_uint(x), __float_as_uint(x), false, false);
  x = fmaxf(__uint_as_float(s[0]), __uint_as_float(s[1]));
  auto t = __builtin_amdgcn_permlane32_swap(__float_as_uint(x), __float_as_uint(x), false, false);
  return fmaxf(__uint_as_float(t[0]), __uint_as_float(t[1]));
}
__device__ __forceinline__ float row_max(float x) {
  x = fmaxf(x, dpp<XOR8>(x));
  x = fmaxf(x, dpp<XOR7>(x));
  x = fmaxf(x, dpp<XOR2>(x));
  x = fmaxf(x, dpp<XOR1>(x));
  return x;
}
__device__ __forceinline__ float row_sum(float x) {
  x += dpp<XOR8>(x);
  x += dpp<XOR7>(x);
  x += dpp<XOR2>(x);
  x += dpp<XOR1>(x);
  return x;
}
__device__ __forceinline__ f32x2 rows_reduce8(const float (&a)[8]) {
  const float u0 = tr32(a[0], a[4]), u1 = tr32(a[1], a[5]), u2 = tr32(a[2], a[6]), u3 = tr32(a[3], a[7]);
  return f32x2{tr16(u0, u2), tr16(u1, u3)};
}
#define PIN() __builtin_amdgcn_sched_barrier(0)

constexpr int NW = 8;
constexpr int NT = NW * 64;
constexpr int NL = 16;
constexpr int R = RING;
constexpr int NSEG = 5;
constexpr int LTOT = NSEG * 32;
constexpr float SCALE = 0.08838834764831845f, LOG2E = 1.4426950408889634f;
constexpr unsigned KVBYTES = (unsigned)NB * NH * SK * DH * 2;
constexpr unsigned WBYTES = (unsigned)DM * HD * 2;

struct Params {
  const uint16_t* x;
  const uint16_t* kA; const uint16_t* vA; const uint16_t* wqA; const uint16_t* woA;
  const uint16_t* kB; const uint16_t* vB; const uint16_t* wqB; const uint16_t* woB;
  const uint16_t* gate;
  float* y;
  float* ml;
  __hip_bfloat16* out;
};

constexpr int L_QP = 0;
constexpr int REC = 132;
constexpr int L_REC = L_QP + 2 * NW * 128;
constexpr int L_O = L_REC + 2 * NW * REC;
constexpr int L_CNT = L_O + 2 * 64;
constexpr int L_END = L_CNT + 4;

struct Ctx {
  rsrc_t kA, vA, kB, vB;
  unsigned offA, offB;
  unsigned vo;
};

template <int L>
__device__ __forceinline__ u32x4 ld_stream(const Ctx& c) {
  constexpr int S = L / 32, PH = (L / 16) & 1, N = L % 16;
  constexpr unsigned rel = (N / 4) * 32768u + (S > 0 ? (S - 1) * 131072u : 0u);
  const unsigned so = (S == 0 ? c.offB : c.offA) + rel;
  const rsrc_t& r = S == 0 ? (PH ? c.vB : c.kB) : (PH ? c.vA : c.kA);
  return __builtin_amdgcn_raw_buffer_load_b128(r, c.vo + (N % 4) * 1024, so, S == 0 ? (PH ? VBAUX : KBAUX) : AAUX);
}
template <bool ISB>
__device__ __forceinline__ u32x4 ld_w(const rsrc_t& r, unsigned vo, unsigned so) {
  return __builtin_amdgcn_raw_buffer_load_b128(r, vo, so, ISB ? WBAUX : WAUX);
}

template <int I>
__device__ __forceinline__ void q_step(float (&qa)[8], const u32x4& w, const u32x4 (&xv)[2]) {
  const unsigned xw = xv[I >> 3][(I >> 1) & 3];
  const float xf = (I & 1) ? bf_hi(xw) : bf_lo(xw);
#pragma unroll
  for (int k = 0; k < 4; ++k) {
    qa[2 * k] = fmaf(xf, bf_lo(w[k]), qa[2 * k]);
    qa[2 * k + 1] = fmaf(xf, bf_hi(w[k]), qa[2 * k + 1]);
  }
}
__device__ __forceinline__ void q_post(const float (&qa)[8], float* lds, int wave, int r, int c) {
  *reinterpret_cast<f32x2*>(lds + wave * 128 + 8 * c + 2 * r) = rows_reduce8(qa);
}
__device__ __forceinline__ u32x4 q_gather(const float* lds, int c) {
  f32x4 a = {0.f, 0.f, 0.f, 0.f}, b = {0.f, 0.f, 0.f, 0.f};
#pragma unroll
  for (int w = 0; w < NW; ++w) {
    a += *reinterpret_cast<const f32x4*>(lds + w * 128 + 8 * c);
    b += *reinterpret_cast<const f32x4*>(lds + w * 128 + 8 * c + 4);
  }
  return u32x4{cvtpk(a[0], a[1]), cvtpk(a[2], a[3]), cvtpk(b[0], b[1]), cvtpk(b[2], b[3])};
}

struct State { float m, l, acc[8]; };

__device__ __forceinline__ float qk(const u32x4& k, const u32x4& q) {
  float d = dot2(k[0], q[0], 0.f);
  d = dot2(k[1], q[1], d);
  d = dot2(k[2], q[2], d);
  return dot2(k[3], q[3], d);
}

__device__ __forceinline__ float softmax_step(State& st, float (&x)[NL], int c) {
#define TR_STEP(HALF, CTRL, BIT)                                          \
  _Pragma("unroll") for (int h = 0; h < (HALF); ++h) {                     \
    const float lo_ = x[h], hi_ = x[h + (HALF)];                           \
    const float t1_ = lo_ + dpp<CTRL>(lo_), t2_ = hi_ + dpp<CTRL>(hi_);    \
    x[h] = (c & (BIT)) ? t2_ : t1_;                                        \
  }
  TR_STEP(8, XOR8, 8) TR_STEP(4, XOR7, 4) TR_STEP(2, XOR2, 2) TR_STEP(1, XOR1, 1)
#undef TR_STEP
  const float s = rbf(rbf(x[0]) * SCALE) * LOG2E;
  const float mn = fmaxf(st.m, row_max(s));
  const float alpha = __builtin_amdgcn_exp2f(st.m - mn);
  const float p = __builtin_amdgcn_exp2f(s - mn);
  st.l = fmaf(st.l, alpha, p);
  st.m = mn;
#pragma unroll
  for (int k = 0; k < 8; ++k) st.acc[k] *= alpha;
  return p;
}

template <int I>
__device__ __forceinline__ void pv_step(State& st, float p, const u32x4& v) {
  const float pb = dpp<BCAST + I>(p);
#pragma unroll
  for (int k = 0; k < 4; ++k) {
    st.acc[2 * k] = fmaf(pb, bf_lo(v[k]), st.acc[2 * k]);
    st.acc[2 * k + 1] = fmaf(pb, bf_hi(v[k]), st.acc[2 * k + 1]);
  }
}

template <int S, int N, int TR0>
struct KPhase {
  static __device__ __forceinline__ void run(const Ctx& c, u32x4 (&ring)[R], const u32x4& q, float (&x)[NL], u32x4 (&W)[NL],
                                             const rsrc_t& rw, unsigned wvo) {
    constexpr int L = S * 32 + N;
    x[N] = qk(ring[L % R], q);
    PIN();
    ring[L % R] = ld_stream<L + R>(c);
    if constexpr (TR0 >= 0 && (N & 1)) W[TR0 + N / 2] = ld_w<S == 1>(rw, wvo, (TR0 + N / 2) * 8 * DM * 2);
    PIN();
    KPhase<S, N + 1, TR0>::run(c, ring, q, x, W, rw, wvo);
  }
};
template <int S, int TR0>
struct KPhase<S, NL, TR0> {
  static __device__ __forceinline__ void run(const Ctx&, u32x4 (&)[R], const u32x4&, float (&)[NL], u32x4 (&)[NL], const rsrc_t&, unsigned) {}
};
template <int S, int N, int TR0>
struct VPhase {
  static __device__ __forceinline__ void run(const Ctx& c, u32x4 (&ring)[R], State& st, float p, u32x4 (&W)[NL], const rsrc_t& rw,
                                             unsigned wvo) {
    constexpr int L = S * 32 + 16 + N;
    pv_step<N>(st, p, ring[L % R]);
    PIN();
    if constexpr (L + R < LTOT) ring[L % R] = ld_stream<L + R>(c);
    if constexpr (TR0 >= 0 && (N & 1)) W[TR0 + N / 2] = ld_w<S == 1>(rw, wvo, (TR0 + N / 2) * 8 * DM * 2);
    PIN();
    VPhase<S, N + 1, TR0>::run(c, ring, st, p, W, rw, wvo);
  }
};
template <int S, int TR0>
struct VPhase<S, NL, TR0> {
  static __device__ __forceinline__ void run(const Ctx&, u32x4 (&)[R], State&, float, u32x4 (&)[NL], const rsrc_t&, unsigned) {}
};

__device__ __forceinline__ void wave_record(State& st, float* lds, int wave, int r, int c, int lane) {
  const float M = xrow_max(st.m);
  const float w = __builtin_amdgcn_exp2f(st.m - M);
  const float L = xrow_sum(row_sum(st.l) * w);
  float a[8];
#pragma unroll
  for (int k = 0; k < 8; ++k) a[k] = st.acc[k] * w;
  float* rec = lds + wave * REC;
  *reinterpret_cast<f32x2*>(rec + 8 * c + 2 * r) = rows_reduce8(a);
  if (lane == 0) *reinterpret_cast<f32x2*>(rec + 128) = f32x2{M, L};
}
__device__ __forceinline__ void wg_merge(float* lds, int tid, float* ml_out) {
  if (tid < 128) {
    const int br = tid >> 6, d = ((tid >> 3) & 7) + 16 * (tid & 7);
    const float* rec = lds + L_REC + br * NW * REC;
    float M = rec[128];
#pragma unroll
    for (int w = 1; w < NW; ++w) M = fmaxf(M, rec[w * REC + 128]);
    float o0 = 0.f, o1 = 0.f, L = 0.f;
#pragma unroll
    for (int w = 0; w < NW; ++w) {
      const float wt = __builtin_amdgcn_exp2f(rec[w * REC + 128] - M);
      o0 = fmaf(wt, rec[w * REC + d], o0);
      o1 = fmaf(wt, rec[w * REC + d + 8], o1);
      L = fmaf(wt, rec[w * REC + 129], L);
    }
    reinterpret_cast<unsigned*>(lds)[L_O + tid] = cvtpk(o0, o1);
    if ((tid & 63) == 0) *reinterpret_cast<float2*>(ml_out + 4 * br) = make_float2(M, L);
  }
}
__device__ __forceinline__ void wo_pair(u32x4 (&W)[NL]) {
#pragma unroll
  for (int j = 0; j < NL / 2; ++j)
#pragma unroll
    for (int k = 0; k < 4; ++k) {
      const unsigned a = W[2 * j][k], b = W[2 * j + 1][k];
      W[2 * j][k] = __builtin_amdgcn_perm(b, a, 0x05040100u);
      W[2 * j + 1][k] = __builtin_amdgcn_perm(b, a, 0x07060302u);
    }
}
__device__ __forceinline__ void out_proj(const u32x4 (&W)[NL], const float* lds, int wave, int lane, float* y_out) {
  const int r8 = lane >> 3, c8 = lane & 7;
  const u32x4 oa = *reinterpret_cast<const u32x4*>(lds + r8 * 8), ob = *reinterpret_cast<const u32x4*>(lds + r8 * 8 + 4);
  float y[8];
#pragma unroll
  for (int k = 0; k < 8; ++k) y[k] = 0.f;
#pragma unroll
  for (int j = 0; j < NL / 2; ++j) {
    const unsigned o = j < 4 ? oa[j & 3] : ob[j & 3];
#pragma unroll
    for (int k = 0; k < 4; ++k) {
      y[2 * k] = dot2(W[2 * j][k], o, y[2 * k]);
      y[2 * k + 1] = dot2(W[2 * j + 1][k], o, y[2 * k + 1]);
    }
  }
  const f32x2 t = rows_reduce8(y);
  const float t0 = t[0] + dpp<XOR8>(t[0]), t1 = t[1] + dpp<XOR8>(t[1]);
  y_out[64 * wave + 8 * c8 + r8] = (lane & 8) ? t1 : t0;
}

__device__ __forceinline__ rsrc_t mk(const void* p, unsigned bytes) {
  return __builtin_amdgcn_make_buffer_rsrc(const_cast<void*>(p), 0, bytes, 0x00020000);
}

struct Pro {
  const Ctx& c; u32x4 (&WA)[NL]; u32x4 (&WB)[NL]; u32x4 (&K0)[NL];
  const rsrc_t& rqA; const rsrc_t& rqB; unsigned qvo;
  template <int P>
  __device__ __forceinline__ void issue() {
    if constexpr (P < 32) {
      if constexpr ((P & 1) == 0) WB[P / 2] = ld_w<true>(rqB, qvo, (P / 2) * HD * 2);
      else K0[P / 2] = ld_stream<P / 2>(c);
    } else if constexpr (P < 56) {
      constexpr int T = (P - 32) / 3, U = (P - 32) % 3;
      if constexpr (U < 2) WA[2 * T + U] = ld_w<false>(rqA, qvo, (2 * T + U) * HD * 2);
      else WB[T] = ld_stream<16 + T>(c);
    } else if constexpr (P < 64) {
      WB[P - 48] = ld_stream<16 + P - 48>(c);
    }
  }
  template <int P0, int P1>
  __device__ __forceinline__ void range() {
    if constexpr (P0 < P1) { issue<P0>(); range<P0 + 1, P1>(); }
  }
};
template <int I>
__device__ __forceinline__ void qb_loop(Pro& pro, float (&qa)[8], const u32x4 (&xv)[2]) {
  if constexpr (I < NL) {
    q_step<I>(qa, pro.WB[I], xv);
    PIN();
    pro.template range<2 * (I + PWIN), 2 * (I + PWIN) + 2>();
    PIN();
    qb_loop<I + 1>(pro, qa, xv);
  }
}
template <int N>
__device__ __forceinline__ void v0_loop(const Ctx& c, Pro& pro, u32x4 (&ring)[R], State& st, float p, float (&qa)[8],
                                        const u32x4 (&xv)[2], float* lds, int wave, int r, int cc, int lane) {
  if constexpr (N < NL) {
    if constexpr (N < 8) { q_step<2 * N>(qa, pro.WA[2 * N], xv); q_step<2 * N + 1>(qa, pro.WA[2 * N + 1], xv); }
    pv_step<N>(st, p, pro.WB[N]);
    PIN();
    if constexpr (N == 7) {
      q_post(qa, lds + L_QP + NW * 128, wave, r, cc);
      __builtin_amdgcn_fence(__ATOMIC_RELEASE, "workgroup");
      if (lane == 0) __hip_atomic_fetch_add(reinterpret_cast<unsigned*>(lds) + L_CNT, 1u, __ATOMIC_RELAXED, __HIP_MEMORY_SCOPE_WORKGROUP);
    }
    if constexpr (N >= NL - R) ring[(32 + N - (NL - R)) % R] = ld_stream<32 + N - (NL - R)>(c);
    PIN();
    v0_loop<N + 1>(c, pro, ring, st, p, qa, xv, lds, wave, r, cc, lane);
  }
}

__device__ __forceinline__ void reset(State& st) {
  st.m = -INFINITY; st.l = 0.f;
#pragma unroll
  for (int k = 0; k < 8; ++k) st.acc[k] = 0.f;
}

__global__ void __launch_bounds__(NT, 2) attn_partial(Params p) {
  __shared__ __attribute__((aligned(16))) float lds[L_END];
  const int bid = blockIdx.x;
  STAMP_DECL;
  STAMP(0);
  const int h = (bid & 7) * 4 + ((bid >> 3) & 3);
  const int j = bid >> 5;
  const int b = j >> 1, half = j & 1;
  const int tid = threadIdx.x, lane = tid & 63;
  const int wave = __builtin_amdgcn_readfirstlane(tid >> 6);
  const int r = lane >> 4, c = lane & 15;

  Ctx cx;
  cx.kA = mk(p.kA, KVBYTES); cx.vA = mk(p.vA, KVBYTES); cx.kB = mk(p.kB, KVBYTES); cx.vB = mk(p.vB, KVBYTES);
  const unsigned bh = (unsigned)(b * NH + h) * SK;
  cx.offB = (bh + (SK - WIN) + half * 512) * (DH * 2) + wave * 4096;
  cx.offA = (bh + half * 2048) * (DH * 2) + wave * 4096;
  cx.vo = lane * 16;
  const rsrc_t rqA = mk(p.wqA, WBYTES), rqB = mk(p.wqB, WBYTES), roA = mk(p.woA, WBYTES), roB = mk(p.woB, WBYTES);
  const unsigned qvo = ((64 * wave + 16 * r) * HD + h * DH + 8 * c) * 2;
  const unsigned ovo = ((h * DH + (lane >> 3)) * DM + 64 * wave + 8 * (lane & 7)) * 2;

  u32x4 WA[NL], WB[NL], K0[NL], ring[R], xv[2];
  {
    const char* xp = reinterpret_cast<const char*>(p.x) + (b * DM + 64 * wave + 16 * r) * 2;
    xv[0] = *reinterpret_cast<const u32x4*>(xp);
    xv[1] = *reinterpret_cast<const u32x4*>(xp + 16);
  }
  if (tid == 0) reinterpret_cast<unsigned*>(lds)[L_CNT] = 0u;
  Pro pro{cx, WA, WB, K0, rqA, rqB, qvo};
  PIN();
  pro.range<0, 2 * PWIN>();
  PIN();
  STAMP(1);

  float* yb = p.y + (size_t)((b * NH + h) * 4 + half) * DM;
  float* mlb = p.ml + (size_t)((b * NH + h) * 4 + half) * 2;

  float qa[8];
#pragma unroll
  for (int k = 0; k < 8; ++k) qa[k] = 0.f;
  qb_loop<0>(pro, qa, xv);
  q_post(qa, lds + L_QP, wave, r, c);
  __syncthreads();
  STAMP(2);
  u32x4 q = q_gather(lds + L_QP, c);
  PIN();
  pro.range<2 * (NL + PWIN), 64>();
  PIN();

  State st;
  float x[NL], pr;
  reset(st);
#pragma unroll
  for (int k = 0; k < 8; ++k) qa[k] = 0.f;
#pragma unroll
  for (int i = 0; i < NL; ++i) x[i] = qk(K0[i], q);
  pr = softmax_step(st, x, c);
  PIN();
  STAMP(3);
  v0_loop<0>(cx, pro, ring, st, pr, qa, xv, lds, wave, r, c, lane);
  STAMP(4);
  wave_record(st, lds + L_REC + NW * REC, wave, r, c, lane);
  reset(st);
  while (__hip_atomic_load(reinterpret_cast<unsigned*>(lds) + L_CNT, __ATOMIC_RELAXED, __HIP_MEMORY_SCOPE_WORKGROUP) < (unsigned)NW)
    __builtin_amdgcn_s_sleep(1);
  __builtin_amdgcn_fence(__ATOMIC_ACQUIRE, "workgroup");
  q = q_gather(lds + L_QP + NW * 128, c);
  PIN();
  STAMP(5);

  KPhase<1, 0, 0>::run(cx, ring, q, x, WB, roB, ovo);
  pr = softmax_step(st, x, c);
  PIN();
  VPhase<1, 0, 8>::run(cx, ring, st, pr, WB, roB, ovo);
  STAMP(6);
  STAMP(7);
  KPhase<2, 0, 0>::run(cx, ring, q, x, WA, roA, ovo);
  pr = softmax_step(st, x, c);
  PIN();
  VPhase<2, 0, 8>::run(cx, ring, st, pr, WA, roA, ovo);
  STAMP(8);
  KPhase<3, 0, -1>::run(cx, ring, q, x, WA, roA, ovo);
  pr = softmax_step(st, x, c);
  wo_pair(WB);
  PIN();
  VPhase<3, 0, -1>::run(cx, ring, st, pr, WA, roA, ovo);
  wo_pair(WA);
  STAMP(9);
  KPhase<4, 0, -1>::run(cx, ring, q, x, WA, roA, ovo);
  pr = softmax_step(st, x, c);
  PIN();
  VPhase<4, 0, -1>::run(cx, ring, st, pr, WA, roA, ovo);
  STAMP(10);

  wave_record(st, lds + L_REC, wave, r, c, lane);
  __syncthreads();
  STAMP(11);
  wg_merge(lds, tid, mlb);
  __syncthreads();
  STAMP(12);
  out_proj(WA, lds + L_O, wave, lane, yb);
  out_proj(WB, lds + L_O + 64, wave, lane, yb + 2 * DM);
  STAMP(13);
  STAMP_FLUSH(0, 14);
}

__global__ void __launch_bounds__(256) combine(Params p) {
  __shared__ __attribute__((aligned(16))) float red[4][32];
  __shared__ float gpart[4];
  STAMP_DECL;
  STAMP(14);
  const int b = blockIdx.x >> 4, cc = blockIdx.x & 15;
  const int tid = threadIdx.x, lane = tid & 63, wave = tid >> 6;
  const int h = tid >> 3, c4 = tid & 7;
  const float* ml = p.ml + (size_t)(b * NH + h) * 8;
  const f32x4 mlA = *reinterpret_cast<const f32x4*>(ml);
  const f32x4 mlB = *reinterpret_cast<const f32x4*>(ml + 4);
  const float* y = p.y + (size_t)(b * NH + h) * 4 * DM + cc * 32 + c4 * 4;
  const f32x4 y0 = *reinterpret_cast<const f32x4*>(y);
  const f32x4 y1 = *reinterpret_cast<const f32x4*>(y + DM);
  const f32x4 y2 = *reinterpret_cast<const f32x4*>(y + 2 * DM);
  const f32x4 y3 = *reinterpret_cast<const f32x4*>(y + 3 * DM);
  const unsigned xw = reinterpret_cast<const unsigned*>(p.x)[b * (DM / 2) + tid];
  const unsigned gw = reinterpret_cast<const unsigned*>(p.gate)[tid];
  float gs = bf_lo(xw) * bf_lo(gw) + bf_hi(xw) * bf_hi(gw);
  gs = xrow_sum(row_sum(gs));
  if (lane == 0) gpart[wave] = gs;

  const float MA = fmaxf(mlA[0], mlA[2]), MB = fmaxf(mlB[0], mlB[2]);
  const float a0 = __builtin_amdgcn_exp2f(mlA[0] - MA), a1 = __builtin_amdgcn_exp2f(mlA[2] - MA);
  const float b0 = __builtin_amdgcn_exp2f(mlB[0] - MB), b1 = __builtin_amdgcn_exp2f(mlB[2] - MB);
  const float iA = 1.f / (a0 * mlA[1] + a1 * mlA[3]);
  const float iB = 1.f / (b0 * mlB[1] + b1 * mlB[3]);
  f32x4 sa = (y0 * a0 + y1 * a1) * iA;
  f32x4 sb = (y2 * b0 + y3 * b1) * iB;
  __syncthreads();
  const float z = gpart[0] + gpart[1] + gpart[2] + gpart[3];
  const float g = 1.f / (1.f + __expf(-z));
  f32x4 s = sa * g + sb * (1.f - g);
#pragma unroll
  for (int k = 0; k < 4; ++k) { float t = s[k]; t += dpp<XOR8>(t); s[k] = xrow_sum(t); }
  if (lane < 8) *reinterpret_cast<f32x4*>(&red[wave][4 * lane]) = s;
  __syncthreads();
  if (tid < 32) {
    const float v = red[0][tid] + red[1][tid] + red[2][tid] + red[3][tid];
    p.out[b * DM + cc * 32 + tid] = __float2bfloat16(v);
  }
  STAMP(15);
  STAMP_FLUSH(14, 16);
}

}

extern "C" void kernel_launch(void* const* d_in, const int* in_sizes, int n_in, void* d_out, int out_size, void* d_ws,
                              size_t ws_size, hipStream_t stream) {
  for (int i = 0; i < n_in; ++i) fprintf(stderr, "in%d %p (%d)\n", i, d_in[i], in_sizes[i]);
  fprintf(stderr, "out %p ws %p %zu\n", d_out, d_ws, ws_size);
  Params p;
  p.x = (const uint16_t*)d_in[0];
  p.kA = (const uint16_t*)d_in[1];
  p.vA = (const uint16_t*)d_in[2];
  p.wqA = (const uint16_t*)d_in[3];
  p.woA = (const uint16_t*)d_in[4];
  p.kB = (const uint16_t*)d_in[6];
  p.vB = (const uint16_t*)d_in[7];
  p.wqB = (const uint16_t*)d_in[8];
  p.woB = (const uint16_t*)d_in[9];
  p.gate = (const uint16_t*)d_in[10];
  p.y = (float*)d_ws;
  p.ml = (float*)((char*)d_ws + (size_t)NB * NH * 4 * DM * 4);
  p.out = (__hip_bfloat16*)d_out;
  attn_partial<<<256, NT, 0, stream>>>(p);
  combine<<<64, 256, 0, stream>>>(p);
}
```

```cpp
#include <hip/hip_runtime.h>
#include <hip/hip_bf16.h>
#include <stdint.h>

#define RING 4
#define WAUX 2
#define WBAUX WAUX
#define KBAUX 2
#define VBAUX 2
#define AAUX 2
#define TLBWARM 0
#define YST 0
#define THRMASK 0x30
#define DRAIN 2
#define HBX 1
#define THRSEL 1
#define ROT 0
#define EARLYB 1
#define K2V 1
#define PWIN 3

#define STAMP(k)
#define STAMP_DECL
#define STAMP_FLUSH(a, b)

namespace {

constexpr int NB = 4, DM = 512, NH = 32, SK = 4096, DH = 128, WIN = 1024;
constexpr int HD = NH * DH;

using u32x4 = __attribute__((ext_vector_type(4))) unsigned;
using f32x4 = __attribute__((ext_vector_type(4))) float;
using f32x2 = __attribute__((ext_vector_type(2))) float;
using bf16x2 = __attribute__((ext_vector_type(2))) __bf16;
using rsrc_t = __amdgpu_buffer_rsrc_t;

__device__ __forceinline__ float dot2(unsigned k, unsigned q, float acc) {
  return __builtin_amdgcn_fdot2_f32_bf16(__builtin_bit_cast(bf16x2, k), __builtin_bit_cast(bf16x2, q), acc, false);
}
__device__ __forceinline__ unsigned cvtpk(float lo, float hi) {
  unsigned r;
  asm volatile("v_cvt_pk_bf16_f32 %0, %1, %2" : "=v"(r) : "v"(lo), "v"(hi));
  return r;
}
template <int CTRL>
__device__ __forceinline__ float dpp(float x) {
  return __builtin_bit_cast(float, __builtin_amdgcn_mov_dpp(__builtin_bit_cast(int, x), CTRL, 0xf, 0xf, true));
}
constexpr int XOR1 = 0xB1, XOR2 = 0x4E, XOR7 = 0x141, XOR8 = 0x128;
constexpr int BCAST = 0x150;

__device__ __forceinline__ float bf_lo(unsigned w) { return __builtin_bit_cast(float, w << 16); }
__device__ __forceinline__ float bf_hi(unsigned w) { return __builtin_bit_cast(float, w & 0xffff0000u); }
__device__ __forceinline__ float rbf(float v) { return bf_lo(cvtpk(v, v)); }

__device__ __forceinline__ float tr32(float u, float w) {
  auto t = __builtin_amdgcn_permlane32_swap(__float_as_uint(u), __float_as_uint(w), false, false);
  return __uint_as_float(t[0]) + __uint_as_float(t[1]);
}
__device__ __forceinline__ float tr16(float u, float w) {
  auto t = __builtin_amdgcn_permlane16_swap(__float_as_uint(u), __float_as_uint(w), false, false);
  return __uint_as_float(t[0]) + __uint_as_float(t[1]);
}
__device__ __forceinline__ float xrow_sum(float x) { x = tr16(x, x); return tr32(x, x); }
__device__ __forceinline__ float xrow_max(float x) {
  auto s = __builtin_amdgcn_permlane16_swap(__float_as_uint(x), __float_as_uint(x), false, false);
  x = fmaxf(__uint_as_float(s[0]), __uint_as_float(s[1]));
  auto t = __builtin_amdgcn_permlane32_swap(__float_as_uint(x), __float_as_uint(x), false, false);
  return fmaxf(__uint_as_float(t[0]), __uint_as_float(t[1]));
}
__device__ __forceinline__ float row_max(float x) {
  x = fmaxf(x, dpp<XOR8>(x));
  x = fmaxf(x, dpp<XOR7>(x));
  x = fmaxf(x, dpp<XOR2>(x));
  x = fmaxf(x, dpp<XOR1>(x));
  return x;
}
__device__ __forceinline__ float row_sum(float x) {
  x += dpp<XOR8>(x);
  x += dpp<XOR7>(x);
  x += dpp<XOR2>(x);
  x += dpp<XOR1>(x);
  return x;
}
__device__ __forceinline__ f32x2 rows_reduce8(const float (&a)[8]) {
  const float u0 = tr32(a[0], a[4]), u1 = tr32(a[1], a[5]), u2 = tr32(a[2], a[6]), u3 = tr32(a[3], a[7]);
  return f32x2{tr16(u0, u2), tr16(u1, u3)};
}
#define PIN() __builtin_amdgcn_sched_barrier(0)
template <int CNT>
__device__ __forceinline__ void throttle(int on) {
  asm volatile("s_cmp_eq_u32 %0, 0\n\ts_cbranch_scc1 1f\n\ts_waitcnt vmcnt(%1)\n1:" ::"s"(on), "i"(CNT) : "scc");
}
constexpr bool thr_on(int F) { return (THRMASK >> (F - 2)) & 1; }

constexpr int NW = 8;
constexpr int NT = NW * 64;
constexpr int NL = 16;
constexpr int R = RING;
constexpr int NSEG = 5;
constexpr int LTOT = NSEG * 32;
constexpr float SCALE = 0.08838834764831845f, LOG2E = 1.4426950408889634f;
constexpr unsigned KVBYTES = (unsigned)NB * NH * SK * DH * 2;
constexpr unsigned WBYTES = (unsigned)DM * HD * 2;

struct Params {
  const uint16_t* x;
  const uint16_t* kA; const uint16_t* vA; const uint16_t* wqA; const uint16_t* woA;
  const uint16_t* kB; const uint16_t* vB; const uint16_t* wqB; const uint16_t* woB;
  const uint16_t* gate;
  float* y;
  float* ml;
  __hip_bfloat16* out;
};

constexpr int L_QP = 0;
constexpr int REC = 132;
constexpr int L_REC = L_QP + 2 * NW * 128;
constexpr int L_O = L_REC + 2 * NW * REC;
constexpr int L_CNT = L_O + 2 * 64;
constexpr int L_END = L_CNT + 4;

struct Ctx {
  rsrc_t kA, vA, kB, vB;
  unsigned offA, offB;
  unsigned vo;
  int thr;
};

template <int L>
__device__ __forceinline__ u32x4 ld_stream(const Ctx& c) {
  constexpr int F = L / 16, N = L % 16;
  constexpr bool ISB = F < 2, ISV = (F == 1 || F >= 6);
  constexpr unsigned rel = (N / 4) * 32768u + (ISB ? 0u : ((F - 2) & 3) * 131072u);
  const unsigned so = (ISB ? c.offB : c.offA) + rel;
  const rsrc_t& r = ISB ? (ISV ? c.vB : c.kB) : (ISV ? c.vA : c.kA);
  return __builtin_amdgcn_raw_buffer_load_b128(r, c.vo + (N % 4) * 1024, so, ISB ? (ISV ? VBAUX : KBAUX) : AAUX);
}
template <bool ISB>
__device__ __forceinline__ u32x4 ld_w(const rsrc_t& r, unsigned vo, unsigned so) {
  return __builtin_amdgcn_raw_buffer_load_b128(r, vo, so, ISB ? WBAUX : WAUX);
}

template <int I>
__device__ __forceinline__ void q_step(float (&qa)[8], const u32x4& w, const u32x4 (&xv)[2]) {
  const unsigned xw = xv[I >> 3][(I >> 1) & 3];
  const float xf = (I & 1) ? bf_hi(xw) : bf_lo(xw);
#pragma unroll
  for (int k = 0; k < 4; ++k) {
    qa[2 * k] = fmaf(xf, bf_lo(w[k]), qa[2 * k]);
    qa[2 * k + 1] = fmaf(xf, bf_hi(w[k]), qa[2 * k + 1]);
  }
}
__device__ __forceinline__ void q_post(const float (&qa)[8], float* lds, int wave, int r, int c) {
  *reinterpret_cast<f32x2*>(lds + wave * 128 + 8 * c + 2 * r) = rows_reduce8(qa);
}
__device__ __forceinline__ u32x4 q_gather(const float* lds, int c) {
  f32x4 a = {0.f, 0.f, 0.f, 0.f}, b = {0.f, 0.f, 0.f, 0.f};
#pragma unroll
  for (int w = 0; w < NW; ++w) {
    a += *reinterpret_cast<const f32x4*>(lds + w * 128 + 8 * c);
    b += *reinterpret_cast<const f32x4*>(lds + w * 128 + 8 * c + 4);
  }
  return u32x4{cvtpk(a[0], a[1]), cvtpk(a[2], a[3]), cvtpk(b[0], b[1]), cvtpk(b[2], b[3])};
}

struct State { float m, l, acc[8]; };

__device__ __forceinline__ float qk(const u32x4& k, const u32x4& q) {
  float d = dot2(k[0], q[0], 0.f);
  d = dot2(k[1], q[1], d);
  d = dot2(k[2], q[2], d);
  return dot2(k[3], q[3], d);
}

__device__ __forceinline__ float score(float (&x)[NL], int c) {
#define TR_STEP(HALF, CTRL, BIT)                                          \
  _Pragma("unroll") for (int h = 0; h < (HALF); ++h) {                     \
    const float lo_ = x[h], hi_ = x[h + (HALF)];                           \
    const float t1_ = lo_ + dpp<CTRL>(lo_), t2_ = hi_ + dpp<CTRL>(hi_);    \
    x[h] = (c & (BIT)) ? t2_ : t1_;                                        \
  }
  TR_STEP(8, XOR8, 8) TR_STEP(4, XOR7, 4) TR_STEP(2, XOR2, 2) TR_STEP(1, XOR1, 1)
#undef TR_STEP
  return rbf(rbf(x[0]) * SCALE) * LOG2E;
}

template <int I>
__device__ __forceinline__ void pv_step(State& st, float p, const u32x4& v) {
  const float pb = dpp<BCAST + I>(p);
#pragma unroll
  for (int k = 0; k < 4; ++k) {
    st.acc[2 * k] = fmaf(pb, bf_lo(v[k]), st.acc[2 * k]);
    st.acc[2 * k + 1] = fmaf(pb, bf_hi(v[k]), st.acc[2 * k + 1]);
  }
}

template <int F, int N, int TR0, bool TRB>
__device__ __forceinline__ void k_loop(const Ctx& c, u32x4 (&ring)[R], const u32x4& q, float (&x)[NL], u32x4 (&W)[NL],
                                       const rsrc_t& rw, unsigned wvo) {
  if constexpr (N < NL) {
    constexpr int L = F * 16 + N;
    x[N] = qk(ring[L % R], q);
    PIN();
    if constexpr (thr_on(F)) throttle<(TR0 >= 0 ? R : R - 2)>(c.thr);
    ring[L % R] = ld_stream<L + R>(c);
    if constexpr (TR0 >= 0 && (N & 1)) W[TR0 + N / 2] = ld_w<TRB>(rw, wvo, (TR0 + N / 2) * 8 * DM * 2);
    PIN();
    k_loop<F, N + 1, TR0, TRB>(c, ring, q, x, W, rw, wvo);
  }
}
template <int F, int N>
__device__ __forceinline__ void v_loop(const Ctx& c, u32x4 (&ring)[R], State& st, float p) {
  if constexpr (N < NL) {
    constexpr int L = F * 16 + N;
    pv_step<N>(st, p, ring[L % R]);
    PIN();
    if constexpr (thr_on(F)) throttle<R - 2>(c.thr);
    ring[L % R] = ld_stream<L + R>(c);
    PIN();
    v_loop<F, N + 1>(c, ring, st, p);
  }
}
template <int N>
__device__ __forceinline__ void vlast_loop(const Ctx& c, u32x4 (&ring)[R], u32x4 (&VL)[NL], State& st, float p) {
  if constexpr (N < NL) {
    constexpr int L0 = LTOT - 16, L = L0 + N;
    if constexpr (N < R) pv_step<N>(st, p, ring[L % R]); else pv_step<N>(st, p, VL[N]);
    PIN();
    constexpr int G = R + DRAIN * N;
    if constexpr (G < NL) VL[G] = ld_stream<L0 + G>(c);
    if constexpr (DRAIN >= 2 && G + 1 < NL) VL[G + 1] = ld_stream<L0 + G + 1>(c);
    if constexpr (DRAIN >= 3 && G + 2 < NL) VL[G + 2] = ld_stream<L0 + G + 2>(c);
    PIN();
    vlast_loop<N + 1>(c, ring, VL, st, p);
  }
}

__device__ __forceinline__ void wave_record(State& st, float* lds, int wave, int r, int c, int lane) {
  const float M = xrow_max(st.m);
  const float w = __builtin_amdgcn_exp2f(st.m - M);
  const float L = xrow_sum(row_sum(st.l) * w);
  float a[8];
#pragma unroll
  for (int k = 0; k < 8; ++k) a[k] = st.acc[k] * w;
  float* rec = lds + wave * REC;
  *reinterpret_cast<f32x2*>(rec + 8 * c + 2 * r) = rows_reduce8(a);
  if (lane == 0) *reinterpret_cast<f32x2*>(rec + 128) = f32x2{M, L};
}
__device__ __forceinline__ void wg_merge(const float* rec, unsigned* o_out, int t, float* ml_out) {
  const int d = (t >> 3) + 16 * (t & 7);
  float M = rec[128];
#pragma unroll
  for (int w = 1; w < NW; ++w) M = fmaxf(M, rec[w * REC + 128]);
  float o0 = 0.f, o1 = 0.f, L = 0.f;
#pragma unroll
  for (int w = 0; w < NW; ++w) {
    const float wt = __builtin_amdgcn_exp2f(rec[w * REC + 128] - M);
    o0 = fmaf(wt, rec[w * REC + d], o0);
    o1 = fmaf(wt, rec[w * REC + d + 8], o1);
    L = fmaf(wt, rec[w * REC + 129], L);
  }
  o_out[t] = cvtpk(o0, o1);
  if (t == 0) *reinterpret_cast<float2*>(ml_out) = make_float2(M, L);
}
__device__ __forceinline__ void wo_pair(u32x4 (&W)[NL]) {
#pragma unroll
  for (int j = 0; j < NL / 2; ++j)
#pragma unroll
    for (int k = 0; k < 4; ++k) {
      const unsigned a = W[2 * j][k], b = W[2 * j + 1][k];
      W[2 * j][k] = __builtin_amdgcn_perm(b, a, 0x05040100u);
      W[2 * j + 1][k] = __builtin_amdgcn_perm(b, a, 0x07060302u);
    }
}
__device__ __forceinline__ void out_proj(const u32x4 (&W)[NL], const float* lds, int wave, int lane, float* y_out) {
  const int r8 = lane >> 3, c8 = lane & 7;
  const u32x4 oa = *reinterpret_cast<const u32x4*>(lds + r8 * 8), ob = *reinterpret_cast<const u32x4*>(lds + r8 * 8 + 4);
  float y[8];
#pragma unroll
  for (int k = 0; k < 8; ++k) y[k] = 0.f;
#pragma unroll
  for (int j = 0; j < NL / 2; ++j) {
    const unsigned o = j < 4 ? oa[j & 3] : ob[j & 3];
#pragma unroll
    for (int k = 0; k < 4; ++k) {
      y[2 * k] = dot2(W[2 * j][k], o, y[2 * k]);
      y[2 * k + 1] = dot2(W[2 * j + 1][k], o, y[2 * k + 1]);
    }
  }
  const f32x2 t = rows_reduce8(y);
  const float t0 = t[0] + dpp<XOR8>(t[0]), t1 = t[1] + dpp<XOR8>(t[1]);
  y_out[64 * wave + 8 * c8 + r8] = (lane & 8) ? t1 : t0;
}

__device__ __forceinline__ rsrc_t mk(const void* p, unsigned bytes) {
  return __builtin_amdgcn_make_buffer_rsrc(const_cast<void*>(p), 0, bytes, 0x00020000);
}

struct Pro {
  const Ctx& c; u32x4 (&WA)[NL]; u32x4 (&WB)[NL]; u32x4 (&K0)[NL]; u32x4 (&V0)[NL]; u32x4 (&ring)[R];
  const rsrc_t& rqA; const rsrc_t& rqB; unsigned qvo, qvoB; int rot;
  template <int P>
  __device__ __forceinline__ void issue() {
    if constexpr (P < 48) {
      constexpr int T = P / 3, U = P % 3;
      const unsigned ro = T * (HD * 2);
      if constexpr (U == 0) WB[T] = ld_w<true>(rqB, qvoB, ro);
      else if constexpr (U == 1) WA[T] = ld_w<false>(rqA, qvo, ro);
      else K0[T] = ld_stream<T>(c);
    } else if constexpr (P < 64) {
      V0[P - 48] = ld_stream<16 + P - 48>(c);
    }
  }
  template <int P0, int P1>
  __device__ __forceinline__ void range() {
    if constexpr (P0 < P1) { issue<P0>(); range<P0 + 1, P1>(); }
  }
};
template <int I>
__device__ __forceinline__ void q_loop(Pro& pro, float (&qa)[8], float (&qb)[8], const u32x4 (&xv)[2]) {
  if constexpr (I < NL) {
    q_step<I>(qb, pro.WB[I], xv);
    q_step<I>(qa, pro.WA[I], xv);
    PIN();
    pro.template range<3 * (I + PWIN), 3 * (I + PWIN) + 3>();
    PIN();
    q_loop<I + 1>(pro, qa, qb, xv);
  }
}
template <int N>
__device__ __forceinline__ void v0_loop(const Ctx& c, Pro& pro, State& st, float p) {
  if constexpr (N < NL) {
    pv_step<N>(st, p, pro.V0[N]);
    PIN();
    if constexpr (N >= NL - R) pro.ring[(32 + N - (NL - R)) % R] = ld_stream<32 + N - (NL - R)>(c);
    PIN();
    v0_loop<N + 1>(c, pro, st, p);
  }
}

__device__ __forceinline__ void lds_arrive(unsigned* cnt, int lane) {
  __builtin_amdgcn_fence(__ATOMIC_RELEASE, "workgroup");
  if (lane == 0) __hip_atomic_fetch_add(cnt, 1u, __ATOMIC_RELAXED, __HIP_MEMORY_SCOPE_WORKGROUP);
}
__device__ __forceinline__ void lds_wait(unsigned* cnt, unsigned n) {
  while (__hip_atomic_load(cnt, __ATOMIC_RELAXED, __HIP_MEMORY_SCOPE_WORKGROUP) < n) __builtin_amdgcn_s_sleep(1);
  __builtin_amdgcn_fence(__ATOMIC_ACQUIRE, "workgroup");
}

__device__ __forceinline__ void reset(State& st) {
  st.m = -INFINITY; st.l = 0.f;
#pragma unroll
  for (int k = 0; k < 8; ++k) st.acc[k] = 0.f;
}

__global__ void __launch_bounds__(NT, 2) attn_partial(Params p) {
  __shared__ __attribute__((aligned(16))) float lds[L_END];
  const int bid = blockIdx.x;
  STAMP_DECL;
  STAMP(0);
  const int h = (bid & 7) * 4 + ((bid >> 3) & 3);
  const int j = bid >> 5;
  const int b = j >> 1, half = j & 1;
  const int tid = threadIdx.x, lane = tid & 63;
  const int wave = __builtin_amdgcn_readfirstlane(tid >> 6);
  const int r = lane >> 4, c = lane & 15;

  Ctx cx;
  cx.kA = mk(p.kA, KVBYTES); cx.vA = mk(p.vA, KVBYTES); cx.kB = mk(p.kB, KVBYTES); cx.vB = mk(p.vB, KVBYTES);
  const unsigned bh = (unsigned)(b * NH + h) * SK;
  const int hB = h ^ HBX;
  cx.offB = ((unsigned)(b * NH + hB) * SK + (SK - WIN) + half * 512) * (DH * 2) + wave * 4096;
  cx.offA = (bh + half * 2048) * (DH * 2) + wave * 4096;
  cx.vo = lane * 16;
  cx.thr = __builtin_amdgcn_readfirstlane(((h & 3) ^ 1) * ((hB & 3) ^ 1));
  const rsrc_t rqA = mk(p.wqA, WBYTES), rqB = mk(p.wqB, WBYTES), roA = mk(p.woA, WBYTES), roB = mk(p.woB, WBYTES);
  const unsigned qvo = ((64 * wave + 16 * r) * HD + h * DH + 8 * c) * 2;
  const unsigned qvoB = ((64 * wave + 16 * r) * HD + hB * DH + 8 * c) * 2;
  const unsigned ovo = ((h * DH + (lane >> 3)) * DM + 64 * wave + 8 * (lane & 7)) * 2;
  const unsigned ovoB = ((hB * DH + (lane >> 3)) * DM + 64 * wave + 8 * (lane & 7)) * 2;

  u32x4 WA[NL], WB[NL], K0[NL], V0[NL], ring[R], xv[2];
  {
    const char* xp = reinterpret_cast<const char*>(p.x) + (b * DM + 64 * wave + 16 * r) * 2;
    xv[0] = *reinterpret_cast<const u32x4*>(xp);
    xv[1] = *reinterpret_cast<const u32x4*>(xp + 16);
  }
  const int rot = 2 * j;
  unsigned* cnt = reinterpret_cast<unsigned*>(lds) + L_CNT;
  if (tid == 0) *cnt = 0u;
  Pro pro{cx, WA, WB, K0, V0, ring, rqA, rqB, qvo, qvoB, rot};
  PIN();
  pro.range<0, 3 * PWIN>();
  PIN();
  STAMP(1);

  float* yb = p.y + (size_t)((b * NH + h) * 4 + half) * DM;
  float* mlb = p.ml + (size_t)((b * NH + h) * 4 + half) * 2;
  float* ybB = p.y + (size_t)((b * NH + hB) * 4 + 2 + half) * DM;
  float* mlbB = p.ml + (size_t)((b * NH + hB) * 4 + 2 + half) * 2;

  float qa[8], qb[8];
#pragma unroll
  for (int k = 0; k < 8; ++k) { qa[k] = 0.f; qb[k] = 0.f; }
  q_loop<0>(pro, qa, qb, xv);
  q_post(qb, lds + L_QP, wave, r, c);
  q_post(qa, lds + L_QP + NW * 128, wave, r, c);
  __syncthreads();
  STAMP(2);
  PIN();
  pro.range<3 * (NL + PWIN), 64>();
  PIN();
  u32x4 q = q_gather(lds + L_QP, c);

  State st;
  float x[NL], pr;
  reset(st);
#pragma unroll
  for (int i = 0; i < NL; ++i) x[i] = qk(K0[i], q);
  {
    const float s0 = score(x, c);
    st.m = row_max(s0);
    pr = __builtin_amdgcn_exp2f(s0 - st.m);
    st.l = pr;
  }
  q = q_gather(lds + L_QP + NW * 128, c);
  PIN();
  STAMP(3);
  v0_loop<0>(cx, pro, st, pr);
  STAMP(4);
  wave_record(st, lds + L_REC + NW * REC, wave, r, c, lane);
  reset(st);
  __syncthreads();
  if (wave == 0) wg_merge(lds + L_REC + NW * REC, reinterpret_cast<unsigned*>(lds) + L_O + 64, lane, mlbB);
  __syncthreads();
  PIN();
  STAMP(5);

  float sc[4];
  k_loop<2, 0, 0, true>(cx, ring, q, x, WB, roB, ovoB);
  sc[0] = score(x, c);
  PIN();
  k_loop<3, 0, 8, true>(cx, ring, q, x, WB, roB, ovoB);
  sc[1] = score(x, c);
  PIN();
  STAMP(6);
  STAMP(7);
  k_loop<4, 0, 0, false>(cx, ring, q, x, WA, roA, ovo);
  sc[2] = score(x, c);
  PIN();
  k_loop<5, 0, 8, false>(cx, ring, q, x, WA, roA, ovo);
  sc[3] = score(x, c);
  st.m = row_max(fmaxf(fmaxf(sc[0], sc[1]), fmaxf(sc[2], sc[3])));
#pragma unroll
  for (int i = 0; i < 4; ++i) sc[i] = __builtin_amdgcn_exp2f(sc[i] - st.m);
  st.l = (sc[0] + sc[1]) + (sc[2] + sc[3]);
  PIN();
  STAMP(8);
  v_loop<6, 0>(cx, ring, st, sc[0]);
  wo_pair(WB);
  out_proj(WB, lds + L_O + 64, wave, lane, ybB);
  PIN();
  v_loop<7, 0>(cx, ring, st, sc[1]);
  wo_pair(WA);
  PIN();
  STAMP(9);
  v_loop<8, 0>(cx, ring, st, sc[2]);
  vlast_loop<0>(cx, ring, K0, st, sc[3]);
  STAMP(10);

  wave_record(st, lds + L_REC, wave, r, c, lane);
  __syncthreads();
  STAMP(11);
  if (wave == 0) wg_merge(lds + L_REC, reinterpret_cast<unsigned*>(lds) + L_O, lane, mlb);
  __syncthreads();
  STAMP(12);
  out_proj(WA, lds + L_O, wave, lane, yb);
  STAMP(13);
  STAMP_FLUSH(0, 14);
}

__global__ void __launch_bounds__(256) combine(Params p) {
  __shared__ __attribute__((aligned(16))) float red[4][32];
  __shared__ float gpart[4];
  STAMP_DECL;
  STAMP(14);
  const int b = blockIdx.x >> 4, cc = blockIdx.x & 15;
  const int tid = threadIdx.x, lane = tid & 63, wave = tid >> 6;
  const int h = tid >> 3, c4 = tid & 7;
  const float* ml = p.ml + (size_t)(b * NH + h) * 8;
  const f32x4 mlA = *reinterpret_cast<const f32x4*>(ml);
  const f32x4 mlB = *reinterpret_cast<const f32x4*>(ml + 4);
  const float* y = p.y + (size_t)(b * NH + h) * 4 * DM + cc * 32 + c4 * 4;
  const f32x4 y0 = *reinterpret_cast<const f32x4*>(y);
  const f32x4 y1 = *reinterpret_cast<const f32x4*>(y + DM);
  const f32x4 y2 = *reinterpret_cast<const f32x4*>(y + 2 * DM);
  const f32x4 y3 = *reinterpret_cast<const f32x4*>(y + 3 * DM);
  const unsigned xw = reinterpret_cast<const unsigned*>(p.x)[b * (DM / 2) + tid];
  const unsigned gw = reinterpret_cast<const unsigned*>(p.gate)[tid];
  float gs = bf_lo(xw) * bf_lo(gw) + bf_hi(xw) * bf_hi(gw);
  gs = xrow_sum(row_sum(gs));
  if (lane == 0) gpart[wave] = gs;

  const float MA = fmaxf(mlA[0], mlA[2]), MB = fmaxf(mlB[0], mlB[2]);
  const float a0 = __builtin_amdgcn_exp2f(mlA[0] - MA), a1 = __builtin_amdgcn_exp2f(mlA[2] - MA);
  const float b0 = __builtin_amdgcn_exp2f(mlB[0] - MB), b1 = __builtin_amdgcn_exp2f(mlB[2] - MB);
  const float iA = 1.f / (a0 * mlA[1] + a1 * mlA[3]);
  const float iB = 1.f / (b0 * mlB[1] + b1 * mlB[3]);
  f32x4 sa = (y0 * a0 + y1 * a1) * iA;
  f32x4 sb = (y2 * b0 + y3 * b1) * iB;
  __syncthreads();
  const float z = gpart[0] + gpart[1] + gpart[2] + gpart[3];
  const float g = 1.f / (1.f + __expf(-z));
  f32x4 s = sa * g + sb * (1.f - g);
#pragma unroll
  for (int k = 0; k < 4; ++k) { float t = s[k]; t += dpp<XOR8>(t); s[k] = xrow_sum(t); }
  if (lane < 8) *reinterpret_cast<f32x4*>(&red[wave][4 * lane]) = s;
  __syncthreads();
  if (tid < 32) {
    const float v = red[0][tid] + red[1][tid] + red[2][tid] + red[3][tid];
    p.out[b * DM + cc * 32 + tid] = __float2bfloat16(v);
  }
  STAMP(15);
  STAMP_FLUSH(14, 16);
}

__global__ void __launch_bounds__(256) combine2(Params p) {
  STAMP_DECL;
  STAMP(14);
  const int b = blockIdx.x >> 4;
  const int tid = threadIdx.x, lane = tid & 63;
  const int col0 = (blockIdx.x & 15) * 32 + (tid >> 6) * 8 + (lane >> 5) * 4;
  const int h = lane & 31;
  const float* ml = p.ml + (size_t)(b * NH + h) * 8;
  const f32x4 mlA = *reinterpret_cast<const f32x4*>(ml);
  const f32x4 mlB = *reinterpret_cast<const f32x4*>(ml + 4);
  const float* y = p.y + (size_t)(b * NH + h) * 4 * DM + col0;
  const f32x4 y0 = *reinterpret_cast<const f32x4*>(y);
  const f32x4 y1 = *reinterpret_cast<const f32x4*>(y + DM);
  const f32x4 y2 = *reinterpret_cast<const f32x4*>(y + 2 * DM);
  const f32x4 y3 = *reinterpret_cast<const f32x4*>(y + 3 * DM);
  const u32x4 xw = reinterpret_cast<const u32x4*>(p.x)[b * (DM / 8) + lane];
  const u32x4 gw = reinterpret_cast<const u32x4*>(p.gate)[lane];
  float gs = 0.f;
#pragma unroll
  for (int k = 0; k < 4; ++k) {
    const unsigned pr = cvtpk(bf_lo(xw[k]) * bf_lo(gw[k]), bf_hi(xw[k]) * bf_hi(gw[k]));
    gs += bf_lo(pr) + bf_hi(pr);
  }
  const float z = rbf(xrow_sum(row_sum(gs)));
  const float g = rbf(1.f / (1.f + __expf(-z)));
  const float omg = rbf(1.f - g);

  const float MA = fmaxf(mlA[0], mlA[2]), MB = fmaxf(mlB[0], mlB[2]);
  const float a0 = __builtin_amdgcn_exp2f(mlA[0] - MA), a1 = __builtin_amdgcn_exp2f(mlA[2] - MA);
  const float b0 = __builtin_amdgcn_exp2f(mlB[0] - MB), b1 = __builtin_amdgcn_exp2f(mlB[2] - MB);
  const float iA = 1.f / (a0 * mlA[1] + a1 * mlA[3]);
  const float iB = 1.f / (b0 * mlB[1] + b1 * mlB[3]);
  const f32x4 sa = (y0 * a0 + y1 * a1) * iA;
  const f32x4 sb = (y2 * b0 + y3 * b1) * iB;
  float u[4];
#pragma unroll
  for (int k = 0; k < 4; ++k) u[k] = tr16(sa[k], sb[k]);
  const int c = lane & 15;
  float v0, v1;
  { const float t1 = u[0] + dpp<XOR8>(u[0]), t2 = u[2] + dpp<XOR8>(u[2]); v0 = (c & 8) ? t2 : t1; }
  { const float t1 = u[1] + dpp<XOR8>(u[1]), t2 = u[3] + dpp<XOR8>(u[3]); v1 = (c & 8) ? t2 : t1; }
  float w;
  { const float t1 = v0 + dpp<XOR7>(v0), t2 = v1 + dpp<XOR7>(v1); w = (c & 4) ? t2 : t1; }
  w += dpp<XOR2>(w);
  w += dpp<XOR1>(w);
  const float t = rbf(rbf(w) * ((lane & 16) ? omg : g));
  const float o = tr16(t, t);
  if ((lane & 19) == 0) p.out[b * DM + col0 + (c >> 2)] = __float2bfloat16(o);
  STAMP(15);
  STAMP_FLUSH(14, 16);
}

}

extern "C" void kernel_launch(void* const* d_in, const int* in_sizes, int n_in, void* d_out, int out_size, void* d_ws,
                              size_t ws_size, hipStream_t stream) {
  Params p;
  p.x = (const uint16_t*)d_in[0];
  p.kA = (const uint16_t*)d_in[1];
  p.vA = (const uint16_t*)d_in[2];
  p.wqA = (const uint16_t*)d_in[3];
  p.woA = (const uint16_t*)d_in[4];
  p.kB = (const uint16_t*)d_in[6];
  p.vB = (const uint16_t*)d_in[7];
  p.wqB = (const uint16_t*)d_in[8];
  p.woB = (const uint16_t*)d_in[9];
  p.gate = (const uint16_t*)d_in[10];
  p.y = (float*)d_ws;
  p.ml = (float*)((char*)d_ws + (size_t)NB * NH * 4 * DM * 4);
  p.out = (__hip_bfloat16*)d_out;
  attn_partial<<<256, NT, 0, stream>>>(p);
  combine2<<<64, 256, 0, stream>>>(p);
}
```

```cpp
#include <hip/hip_runtime.h>
#include <hip/hip_bf16.h>
#include <stdint.h>

#define RING 4
#define WAUX 2
#define WBAUX WAUX
#define KBAUX 2
#define VBAUX 2
#define AAUX 2
#define TLBWARM 0
#define YST 0
#define THRMASK 0x00
#define DRAIN 2
#define HBX 1
#define THRSEL 1
#define ROT 0
#define EARLYB 1
#define K2V 1
#define QUAD 0
#define TAILN 2
#define DPOST 8
#define QPACE 1
#define THRC 3
#define THR_L0 96
#define THR_N 18
#define THR_N31 9
#define PWIN 2

#define STAMP(k)
#define STAMP_DECL
#define STAMP_FLUSH(a, b)

namespace {

constexpr int NB = 4, DM = 512, NH = 32, SK = 4096, DH = 128, WIN = 1024;
constexpr int HD = NH * DH;

using u32x4 = __attribute__((ext_vector_type(4))) unsigned;
using f32x4 = __attribute__((ext_vector_type(4))) float;
using f32x2 = __attribute__((ext_vector_type(2))) float;
using bf16x2 = __attribute__((ext_vector_type(2))) __bf16;
using rsrc_t = __amdgpu_buffer_rsrc_t;

__device__ __forceinline__ float dot2(unsigned k, unsigned q, float acc) {
  return __builtin_amdgcn_fdot2_f32_bf16(__builtin_bit_cast(bf16x2, k), __builtin_bit_cast(bf16x2, q), acc, false);
}
__device__ __forceinline__ unsigned cvtpk(float lo, float hi) {
  unsigned r;
  asm volatile("v_cvt_pk_bf16_f32 %0, %1, %2" : "=v"(r) : "v"(lo), "v"(hi));
  return r;
}
template <int CTRL>
__device__ __forceinline__ float dpp(float x) {
  return __builtin_bit_cast(float, __builtin_amdgcn_mov_dpp(__builtin_bit_cast(int, x), CTRL, 0xf, 0xf, true));
}
constexpr int XOR1 = 0xB1, XOR2 = 0x4E, XOR7 = 0x141, XOR8 = 0x128;
constexpr int BCAST = 0x150;

__device__ __forceinline__ float bf_lo(unsigned w) { return __builtin_bit_cast(float, w << 16); }
__device__ __forceinline__ float bf_hi(unsigned w) { return __builtin_bit_cast(float, w & 0xffff0000u); }
__device__ __forceinline__ float rbf(float v) { return bf_lo(cvtpk(v, v)); }

__device__ __forceinline__ float tr32(float u, float w) {
  auto t = __builtin_amdgcn_permlane32_swap(__float_as_uint(u), __float_as_uint(w), false, false);
  return __uint_as_float(t[0]) + __uint_as_float(t[1]);
}
__device__ __forceinline__ float tr16(float u, float w) {
  auto t = __builtin_amdgcn_permlane16_swap(__float_as_uint(u), __float_as_uint(w), false, false);
  return __uint_as_float(t[0]) + __uint_as_float(t[1]);
}
__device__ __forceinline__ float xrow_sum(float x) { x = tr16(x, x); return tr32(x, x); }
__device__ __forceinline__ float xrow_max(float x) {
  auto s = __builtin_amdgcn_permlane16_swap(__float_as_uint(x), __float_as_uint(x), false, false);
  x = fmaxf(__uint_as_float(s[0]), __uint_as_float(s[1]));
  auto t = __builtin_amdgcn_permlane32_swap(__float_as_uint(x), __float_as_uint(x), false, false);
  return fmaxf(__uint_as_float(t[0]), __uint_as_float(t[1]));
}
__device__ __forceinline__ float row_max(float x) {
  x = fmaxf(x, dpp<XOR8>(x));
  x = fmaxf(x, dpp<XOR7>(x));
  x = fmaxf(x, dpp<XOR2>(x));
  x = fmaxf(x, dpp<XOR1>(x));
  return x;
}
__device__ __forceinline__ float row_sum(float x) {
  x += dpp<XOR8>(x);
  x += dpp<XOR7>(x);
  x += dpp<XOR2>(x);
  x += dpp<XOR1>(x);
  return x;
}
__device__ __forceinline__ f32x2 rows_reduce8(const float (&a)[8]) {
  const float u0 = tr32(a[0], a[4]), u1 = tr32(a[1], a[5]), u2 = tr32(a[2], a[6]), u3 = tr32(a[3], a[7]);
  return f32x2{tr16(u0, u2), tr16(u1, u3)};
}
#define PIN() __builtin_amdgcn_sched_barrier(0)
template <int CNT, int STEP>
__device__ __forceinline__ void throttle(int n) {
  asm volatile("s_cmp_le_u32 %0, %2\n\ts_cbranch_scc1 1f\n\ts_waitcnt vmcnt(%1)\n1:" ::"s"(n), "i"(CNT), "i"(STEP) : "scc");
}
constexpr bool thr_on(int F) { return (THRMASK >> (F - 2)) & 1; }

constexpr int NW = 8;
constexpr int NT = NW * 64;
constexpr int NL = 16;
constexpr int R = RING;
constexpr int NSEG = 5;
constexpr int LTOT = NSEG * 32;
constexpr float SCALE = 0.08838834764831845f, LOG2E = 1.4426950408889634f;
constexpr unsigned KVBYTES = (unsigned)NB * NH * SK * DH * 2;
constexpr unsigned WBYTES = (unsigned)DM * HD * 2;

struct Params {
  const uint16_t* x;
  const uint16_t* kA; const uint16_t* vA; const uint16_t* wqA; const uint16_t* woA;
  const uint16_t* kB; const uint16_t* vB; const uint16_t* wqB; const uint16_t* woB;
  const uint16_t* gate;
  float* y;
  float* ml;
  __hip_bfloat16* out;
};

constexpr int L_QP = 0;
constexpr int REC = 132;
constexpr int L_REC = L_QP + 2 * NW * 128;
constexpr int L_O = L_REC + 2 * NW * REC;
constexpr int L_OB = L_O + 64;
constexpr int L_CNT = L_OB + NW * 64;
constexpr int L_END = L_CNT + 4;

struct Ctx {
  rsrc_t kA, vA, kB, vB;
  unsigned offA, offB;
  unsigned vo;
  int thr;
};

template <int L>
__device__ __forceinline__ u32x4 ld_stream(const Ctx& c) {
  constexpr int F = L / 16, N = L % 16;
  constexpr bool ISB = F < 2, ISV = (F == 1 || F >= 6);
  constexpr unsigned rel = (N / 4) * 32768u + (ISB ? 0u : ((F - 2) & 3) * 131072u);
  const unsigned so = (ISB ? c.offB : c.offA) + rel;
  const rsrc_t& r = ISB ? (ISV ? c.vB : c.kB) : (ISV ? c.vA : c.kA);
  return __builtin_amdgcn_raw_buffer_load_b128(r, c.vo + (N % 4) * 1024, so, ISB ? (ISV ? VBAUX : KBAUX) : AAUX);
}
template <bool ISB>
__device__ __forceinline__ u32x4 ld_w(const rsrc_t& r, unsigned vo, unsigned so) {
  return __builtin_amdgcn_raw_buffer_load_b128(r, vo, so, ISB ? WBAUX : WAUX);
}

template <int I>
__device__ __forceinline__ void q_step(float (&qa)[8], const u32x4& w, const u32x4 (&xv)[2]) {
  const unsigned xw = xv[I >> 3][(I >> 1) & 3];
  const float xf = (I & 1) ? bf_hi(xw) : bf_lo(xw);
#pragma unroll
  for (int k = 0; k < 4; ++k) {
    qa[2 * k] = fmaf(xf, bf_lo(w[k]), qa[2 * k]);
    qa[2 * k + 1] = fmaf(xf, bf_hi(w[k]), qa[2 * k + 1]);
  }
}
__device__ __forceinline__ void q_post(const float (&qa)[8], float* lds, int wave, int r, int c) {
  *reinterpret_cast<f32x2*>(lds + wave * 128 + 8 * c + 2 * r) = rows_reduce8(qa);
}
__device__ __forceinline__ u32x4 q_gather(const float* lds, int c) {
  f32x4 a = {0.f, 0.f, 0.f, 0.f}, b = {0.f, 0.f, 0.f, 0.f};
#pragma unroll
  for (int w = 0; w < NW; ++w) {
    a += *reinterpret_cast<const f32x4*>(lds + w * 128 + 8 * c);
    b += *reinterpret_cast<const f32x4*>(lds + w * 128 + 8 * c + 4);
  }
  return u32x4{cvtpk(a[0], a[1]), cvtpk(a[2], a[3]), cvtpk(b[0], b[1]), cvtpk(b[2], b[3])};
}

struct State { float m, l, acc[8]; };

__device__ __forceinline__ float qk(const u32x4& k, const u32x4& q) {
  float d = dot2(k[0], q[0], 0.f);
  d = dot2(k[1], q[1], d);
  d = dot2(k[2], q[2], d);
  return dot2(k[3], q[3], d);
}

__device__ __forceinline__ float score(float (&x)[NL], int c) {
#define TR_STEP(HALF, CTRL, BIT)                                          \
  _Pragma("unroll") for (int h = 0; h < (HALF); ++h) {                     \
    const float lo_ = x[h], hi_ = x[h + (HALF)];                           \
    const float t1_ = lo_ + dpp<CTRL>(lo_), t2_ = hi_ + dpp<CTRL>(hi_);    \
    x[h] = (c & (BIT)) ? t2_ : t1_;                                        \
  }
  TR_STEP(8, XOR8, 8) TR_STEP(4, XOR7, 4) TR_STEP(2, XOR2, 2) TR_STEP(1, XOR1, 1)
#undef TR_STEP
  return rbf(rbf(x[0]) * SCALE) * LOG2E;
}

template <int I>
__device__ __forceinline__ void pv_step(State& st, float p, const u32x4& v) {
  const float pb = dpp<BCAST + I>(p);
#pragma unroll
  for (int k = 0; k < 4; ++k) {
    st.acc[2 * k] = fmaf(pb, bf_lo(v[k]), st.acc[2 * k]);
    st.acc[2 * k + 1] = fmaf(pb, bf_hi(v[k]), st.acc[2 * k + 1]);
  }
}

__device__ __forceinline__ void wave_record(State& st, float* lds, int wave, int r, int c, int lane) {
  const float M = xrow_max(st.m);
  const float w = __builtin_amdgcn_exp2f(st.m - M);
  const float L = xrow_sum(row_sum(st.l) * w);
  float a[8];
#pragma unroll
  for (int k = 0; k < 8; ++k) a[k] = st.acc[k] * w;
  float* rec = lds + wave * REC;
  *reinterpret_cast<f32x2*>(rec + 8 * c + 2 * r) = rows_reduce8(a);
  if (lane == 0) *reinterpret_cast<f32x2*>(rec + 128) = f32x2{M, L};
}
__device__ __forceinline__ void wg_merge(const float* rec, unsigned* o_out, int t, float* ml_out, bool store_ml) {
  const int d = (t >> 3) + 16 * (t & 7);
  float M = rec[128];
#pragma unroll
  for (int w = 1; w < NW; ++w) M = fmaxf(M, rec[w * REC + 128]);
  float o0 = 0.f, o1 = 0.f, L = 0.f;
#pragma unroll
  for (int w = 0; w < NW; ++w) {
    const float wt = __builtin_amdgcn_exp2f(rec[w * REC + 128] - M);
    o0 = fmaf(wt, rec[w * REC + d], o0);
    o1 = fmaf(wt, rec[w * REC + d + 8], o1);
    L = fmaf(wt, rec[w * REC + 129], L);
  }
  o_out[t] = cvtpk(o0, o1);
  if (t == 0 && store_ml) *reinterpret_cast<float2*>(ml_out) = make_float2(M, L);
}
__device__ __forceinline__ void wo_pair(u32x4 (&W)[NL]) {
#pragma unroll
  for (int j = 0; j < NL / 2; ++j)
#pragma unroll
    for (int k = 0; k < 4; ++k) {
      const unsigned a = W[2 * j][k], b = W[2 * j + 1][k];
      W[2 * j][k] = __builtin_amdgcn_perm(b, a, 0x05040100u);
      W[2 * j + 1][k] = __builtin_amdgcn_perm(b, a, 0x07060302u);
    }
}
__device__ __forceinline__ void out_proj(const u32x4 (&W)[NL], const float* lds, int wave, int lane, float* y_out) {
  const int r8 = lane >> 3, c8 = lane & 7;
  const u32x4 oa = *reinterpret_cast<const u32x4*>(lds + r8 * 8), ob = *reinterpret_cast<const u32x4*>(lds + r8 * 8 + 4);
  float y[8];
#pragma unroll
  for (int k = 0; k < 8; ++k) y[k] = 0.f;
#pragma unroll
  for (int j = 0; j < NL / 2; ++j) {
    const unsigned o = j < 4 ? oa[j & 3] : ob[j & 3];
#pragma unroll
    for (int k = 0; k < 4; ++k) {
      y[2 * k] = dot2(W[2 * j][k], o, y[2 * k]);
      y[2 * k + 1] = dot2(W[2 * j + 1][k], o, y[2 * k + 1]);
    }
  }
  const f32x2 t = rows_reduce8(y);
  const float t0 = t[0] + dpp<XOR8>(t[0]), t1 = t[1] + dpp<XOR8>(t[1]);
  y_out[64 * wave + 8 * c8 + r8] = (lane & 8) ? t1 : t0;
}

__device__ __forceinline__ rsrc_t mk(const void* p, unsigned bytes) {
  return __builtin_amdgcn_make_buffer_rsrc(const_cast<void*>(p), 0, bytes, 0x00020000);
}

__device__ __forceinline__ void lds_arrive(unsigned addr) {
  asm volatile("s_waitcnt lgkmcnt(0)\n\tds_add_u32 %0, %1" ::"v"(addr), "v"(1u) : "memory");
}
template <int N>
__device__ __forceinline__ void lds_wait(unsigned addr) {
  unsigned v, sv;
  asm volatile(
      "1:\n\tds_read_b32 %0, %2\n\ts_waitcnt lgkmcnt(0)\n\tv_readfirstlane_b32 %1, %0\n\ts_nop 1\n\t"
      "s_cmp_ge_u32 %1, %3\n\ts_cbranch_scc1 2f\n\ts_sleep 1\n\ts_branch 1b\n2:"
      : "=&v"(v), "=&s"(sv) : "v"(addr), "i"(N) : "memory", "scc");
}
template <int CNT>
__device__ __forceinline__ void pace() { asm volatile("s_waitcnt vmcnt(%0)" ::"i"(CNT)); }

struct Str {
  const Ctx& c;
  const rsrc_t& rqA; const rsrc_t& rqB; unsigned qvo, qvoB;
  u32x4 S[LTOT], QB[NL], QA[NL];
  template <int P>
  __device__ __forceinline__ void issue() {
    if constexpr (P < 96) {
      constexpr int T = (P % 48) / 3, U = P % 3;
      if constexpr (U == 0) {
        if constexpr (P < 48) QB[T] = ld_w<true>(rqB, qvoB, T * (HD * 2));
        else QA[T] = ld_w<false>(rqA, qvo, T * (HD * 2));
      } else {
        constexpr int L = (P < 48 ? 0 : 32) + 2 * T + (U - 1);
        S[L] = ld_stream<L>(c);
      }
    } else if constexpr (P - 32 < LTOT) {
      S[P - 32] = ld_stream<P - 32>(c);
    }
  }
  template <int P0, int P1>
  __device__ __forceinline__ void range() {
    if constexpr (P0 < P1) { issue<P0>(); range<P0 + 1, P1>(); }
  }
  template <int P0, int P1>
  __device__ __forceinline__ void paced() {
    if constexpr (P0 < P1) { pace<R - 1>(); issue<P0>(); PIN(); paced<P0 + 1, P1>(); }
  }
};
constexpr int p_after(int G) {
  const int full = 3 * (G + PWIN);
  return full <= 96 ? full : 96 + (full - 96) / 3 * TAILN;
}
template <int I0, int I1>
__device__ __forceinline__ void qb_steps(Str& s, float (&qb)[8], const u32x4 (&xv)[2]) {
  if constexpr (I0 < I1) {
    q_step<I0>(qb, s.QB[I0], xv);
    PIN();
    s.template range<p_after(I0), p_after(I0 + 1)>();
    PIN();
    qb_steps<I0 + 1, I1>(s, qb, xv);
  }
}
template <int I0, int I1>
__device__ __forceinline__ void qa_steps(Str& s, float (&qa)[8], const u32x4 (&xv)[2]) {
  if constexpr (I0 < I1) {
    q_step<I0>(qa, s.QA[I0], xv);
    PIN();
    if constexpr (QPACE) pace<p_after(16 + I0) - (48 + 3 * I0) - 1>();
    s.template range<p_after(16 + I0), p_after(17 + I0)>();
    PIN();
    qa_steps<I0 + 1, I1>(s, qa, xv);
  }
}
template <int L0, int N0, int N1>
__device__ __forceinline__ void pv_steps(Str& s, State& st, float p) {
  if constexpr (N0 < N1) { pv_step<N0>(st, p, s.S[L0 + N0]); pv_steps<L0, N0 + 1, N1>(s, st, p); }
}

constexpr int I_START = p_after(32) - 32 + DPOST;
constexpr int issued_at(int L) {
  const int fast = I_START + (L - 32) / 4;
  const int lim = L + R;
  const int v = fast > lim ? fast : lim;
  if (L >= LTOT - 16) { const int d = LTOT - 16 + R + DRAIN * (L - (LTOT - 16)); return d > LTOT ? LTOT : d; }
  return v > LTOT ? LTOT : v;
}
constexpr int WO_L0 = 80;
template <int L>
__device__ __forceinline__ void refill(Str& s, u32x4 (&WA)[NL], u32x4 (&WB)[NL], const rsrc_t& roA, const rsrc_t& roB,
                                       unsigned ovo, unsigned ovoB) {
  constexpr int A = issued_at(L), B = issued_at(L + 1);
  constexpr bool TR = L >= WO_L0 && L < WO_L0 + 64;
  PIN();
  if constexpr (B > A) {
    if constexpr (A > L + R) pace<R - 1>();
    else if constexpr (L >= THR_L0 && L < THR_L0 + THR_N && L < LTOT - 16) throttle<R - 2, L - THR_L0>(s.c.thr);
    s.template range<A + 32, B + 32>();
  }
  if constexpr (TR && (L & 1)) {
    constexpr int w = (L - WO_L0) / 2;
    if constexpr (w < 16) WB[w] = ld_w<true>(roB, ovoB, w * 8 * DM * 2);
    else WA[w - 16] = ld_w<false>(roA, ovo, (w - 16) * 8 * DM * 2);
  }
  PIN();
}
template <int F, int N>
__device__ __forceinline__ void k_loop(Str& s, const u32x4& q, float (&x)[NL], u32x4 (&WA)[NL], u32x4 (&WB)[NL], const rsrc_t& roA,
                                       const rsrc_t& roB, unsigned ovo, unsigned ovoB) {
  if constexpr (N < NL) {
    x[N] = qk(s.S[F * 16 + N], q);
    refill<F * 16 + N>(s, WA, WB, roA, roB, ovo, ovoB);
    k_loop<F, N + 1>(s, q, x, WA, WB, roA, roB, ovo, ovoB);
  }
}
template <int F, int N>
__device__ __forceinline__ void v_loop(Str& s, State& st, float p, u32x4 (&WA)[NL], u32x4 (&WB)[NL], const rsrc_t& roA,
                                       const rsrc_t& roB, unsigned ovo, unsigned ovoB) {
  if constexpr (N < NL) {
    pv_step<N>(st, p, s.S[F * 16 + N]);
    refill<F * 16 + N>(s, WA, WB, roA, roB, ovo, ovoB);
    v_loop<F, N + 1>(s, st, p, WA, WB, roA, roB, ovo, ovoB);
  }
}

__device__ __forceinline__ void reset(State& st) {
  st.m = -INFINITY; st.l = 0.f;
#pragma unroll
  for (int k = 0; k < 8; ++k) st.acc[k] = 0.f;
}

__global__ void __launch_bounds__(NT, 2) attn_partial(Params p) {
  __shared__ __attribute__((aligned(16))) float lds[L_END];
  const int bid = blockIdx.x;
  STAMP_DECL;
  STAMP(0);
  const int h = (bid & 7) * 4 + ((bid >> 3) & 3);
  const int hB = h ^ HBX;
  const int j = bid >> 5;
  const int b = j >> 1, half = j & 1;
  const int tid = threadIdx.x, lane = tid & 63;
  const int wave = __builtin_amdgcn_readfirstlane(tid >> 6);
  const int r = lane >> 4, c = lane & 15;

  Ctx cx;
  cx.kA = mk(p.kA, KVBYTES); cx.vA = mk(p.vA, KVBYTES); cx.kB = mk(p.kB, KVBYTES); cx.vB = mk(p.vB, KVBYTES);
  cx.offB = ((unsigned)(b * NH + hB) * SK + (SK - WIN) + half * 512) * (DH * 2) + wave * 4096;
  cx.offA = ((unsigned)(b * NH + h) * SK + half * 2048) * (DH * 2) + wave * 4096;
  cx.vo = lane * 16;
  cx.thr = __builtin_amdgcn_readfirstlane(((h & 3) == 1 || (hB & 3) == 1) ? 0 : ((h | 1) == 31 ? THR_N31 : THR_N));
  const rsrc_t rqA = mk(p.wqA, WBYTES), rqB = mk(p.wqB, WBYTES), roA = mk(p.woA, WBYTES), roB = mk(p.woB, WBYTES);
  const unsigned qvo = ((64 * wave + 16 * r) * HD + h * DH + 8 * c) * 2;
  const unsigned qvoB = ((64 * wave + 16 * r) * HD + hB * DH + 8 * c) * 2;
  const unsigned ovo = ((h * DH + (lane >> 3)) * DM + 64 * wave + 8 * (lane & 7)) * 2;
  const unsigned ovoB = ((hB * DH + (lane >> 3)) * DM + 64 * wave + 8 * (lane & 7)) * 2;

  u32x4 WA[NL], WB[NL], xv[2];
  {
    const char* xp = reinterpret_cast<const char*>(p.x) + (b * DM + 64 * wave + 16 * r) * 2;
    xv[0] = *reinterpret_cast<const u32x4*>(xp);
    xv[1] = *reinterpret_cast<const u32x4*>(xp + 16);
  }
  unsigned* cnt = reinterpret_cast<unsigned*>(lds) + L_CNT;
  const unsigned cnt_addr = (unsigned)(size_t)(__attribute__((address_space(3))) void*)cnt;
  if (tid == 0) *cnt = 0u;
  Str s{cx, rqA, rqB, qvo, qvoB};
  PIN();
  s.range<0, p_after(0)>();
  PIN();
  STAMP(1);

  float* yb = p.y + (size_t)((b * NH + h) * 4 + half) * DM;
  float* mlb = p.ml + (size_t)((b * NH + h) * 4 + half) * 2;
  float* ybB = p.y + (size_t)((b * NH + hB) * 4 + 2 + half) * DM;
  float* mlbB = p.ml + (size_t)((b * NH + hB) * 4 + 2 + half) * 2;

  float qa[8];
#pragma unroll
  for (int k = 0; k < 8; ++k) qa[k] = 0.f;
  qb_steps<0, NL>(s, qa, xv);
  q_post(qa, lds + L_QP, wave, r, c);
  __syncthreads();
  STAMP(2);

  u32x4 q = q_gather(lds + L_QP, c);
  State st;
  float x[NL], pr;
  reset(st);
#pragma unroll
  for (int k = 0; k < 8; ++k) qa[k] = 0.f;
#pragma unroll
  for (int i = 0; i < NL; ++i) x[i] = qk(s.S[i], q);
  PIN();
  qa_steps<0, 1>(s, qa, xv);
  {
    const float s0 = score(x, c);
    st.m = row_max(s0);
    pr = __builtin_amdgcn_exp2f(s0 - st.m);
    st.l = pr;
  }
  PIN();
  STAMP(3);
  qa_steps<1, 2>(s, qa, xv);
  pv_steps<16, 0, 4>(s, st, pr);   PIN(); qa_steps<2, 3>(s, qa, xv);
  pv_steps<16, 4, 8>(s, st, pr);   PIN(); qa_steps<3, 4>(s, qa, xv);
  pv_steps<16, 8, 12>(s, st, pr);  PIN(); qa_steps<4, 5>(s, qa, xv);
  pv_steps<16, 12, 16>(s, st, pr); PIN(); qa_steps<5, 6>(s, qa, xv);
  wave_record(st, lds + L_REC + NW * REC, wave, r, c, lane);
  reset(st);
  PIN();
  STAMP(4);
  qa_steps<6, NL>(s, qa, xv);
  q_post(qa, lds + L_QP + NW * 128, wave, r, c);
  lds_arrive(cnt_addr);
  PIN();
  s.paced<p_after(32), p_after(32) + DPOST>();
  lds_wait<64 * NW>(cnt_addr);
  STAMP(5);
  q = q_gather(lds + L_QP + NW * 128, c);
  PIN();

  float sc[4];
  k_loop<2, 0>(s, q, x, WA, WB, roA, roB, ovo, ovoB);
  sc[0] = score(x, c);
  k_loop<3, 0>(s, q, x, WA, WB, roA, roB, ovo, ovoB);
  sc[1] = score(x, c);
  STAMP(6);
  k_loop<4, 0>(s, q, x, WA, WB, roA, roB, ovo, ovoB);
  sc[2] = score(x, c);
  wg_merge(lds + L_REC + NW * REC, reinterpret_cast<unsigned*>(lds) + L_OB + wave * 64, lane, mlbB, wave == 0);
  STAMP(7);
  k_loop<5, 0>(s, q, x, WA, WB, roA, roB, ovo, ovoB);
  sc[3] = score(x, c);
  st.m = row_max(fmaxf(fmaxf(sc[0], sc[1]), fmaxf(sc[2], sc[3])));
#pragma unroll
  for (int i = 0; i < 4; ++i) sc[i] = __builtin_amdgcn_exp2f(sc[i] - st.m);
  st.l = (sc[0] + sc[1]) + (sc[2] + sc[3]);
  STAMP(8);
  v_loop<6, 0>(s, st, sc[0], WA, WB, roA, roB, ovo, ovoB);
  v_loop<7, 0>(s, st, sc[1], WA, WB, roA, roB, ovo, ovoB);
  wo_pair(WB);
  out_proj(WB, lds + L_OB + wave * 64, wave, lane, ybB);
  STAMP(9);
  v_loop<8, 0>(s, st, sc[2], WA, WB, roA, roB, ovo, ovoB);
  v_loop<9, 0>(s, st, sc[3], WA, WB, roA, roB, ovo, ovoB);
  STAMP(10);

  wave_record(st, lds + L_REC, wave, r, c, lane);
  wo_pair(WA);
  __syncthreads();
  STAMP(11);
  if (wave == 0) wg_merge(lds + L_REC, reinterpret_cast<unsigned*>(lds) + L_O, lane, mlb, true);
  __syncthreads();
  STAMP(12);
  out_proj(WA, lds + L_O, wave, lane, yb);
  STAMP(13);
  STAMP_FLUSH(0, 14);
}

__global__ void __launch_bounds__(256) combine(Params p) {
  __shared__ __attribute__((aligned(16))) float red[4][32];
  __shared__ float gpart[4];
  STAMP_DECL;
  STAMP(14);
  const int b = blockIdx.x >> 4, cc = blockIdx.x & 15;
  const int tid = threadIdx.x, lane = tid & 63, wave = tid >> 6;
  const int h = tid >> 3, c4 = tid & 7;
  const float* ml = p.ml + (size_t)(b * NH + h) * 8;
  const f32x4 mlA = *reinterpret_cast<const f32x4*>(ml);
  const f32x4 mlB = *reinterpret_cast<const f32x4*>(ml + 4);
  const float* y = p.y + (size_t)(b * NH + h) * 4 * DM + cc * 32 + c4 * 4;
  const f32x4 y0 = *reinterpret_cast<const f32x4*>(y);
  const f32x4 y1 = *reinterpret_cast<const f32x4*>(y + DM);
  const f32x4 y2 = *reinterpret_cast<const f32x4*>(y + 2 * DM);
  const f32x4 y3 = *reinterpret_cast<const f32x4*>(y + 3 * DM);
  const unsigned xw = reinterpret_cast<const unsigned*>(p.x)[b * (DM / 2) + tid];
  const unsigned gw = reinterpret_cast<const unsigned*>(p.gate)[tid];
  float gs = bf_lo(xw) * bf_lo(gw) + bf_hi(xw) * bf_hi(gw);
  gs = xrow_sum(row_sum(gs));
  if (lane == 0) gpart[wave] = gs;

  const float MA = fmaxf(mlA[0], mlA[2]), MB = fmaxf(mlB[0], mlB[2]);
  const float a0 = __builtin_amdgcn_exp2f(mlA[0] - MA), a1 = __builtin_amdgcn_exp2f(mlA[2] - MA);
  const float b0 = __builtin_amdgcn_exp2f(mlB[0] - MB), b1 = __builtin_amdgcn_exp2f(mlB[2] - MB);
  const float iA = 1.f / (a0 * mlA[1] + a1 * mlA[3]);
  const float iB = 1.f / (b0 * mlB[1] + b1 * mlB[3]);
  f32x4 sa = (y0 * a0 + y1 * a1) * iA;
  f32x4 sb = (y2 * b0 + y3 * b1) * iB;
  __syncthreads();
  const float z = gpart[0] + gpart[1] + gpart[2] + gpart[3];
  const float g = 1.f / (1.f + __expf(-z));
  f32x4 s = sa * g + sb * (1.f - g);
#pragma unroll
  for (int k = 0; k < 4; ++k) { float t = s[k]; t += dpp<XOR8>(t); s[k] = xrow_sum(t); }
  if (lane < 8) *reinterpret_cast<f32x4*>(&red[wave][4 * lane]) = s;
  __syncthreads();
  if (tid < 32) {
    const float v = red[0][tid] + red[1][tid] + red[2][tid] + red[3][tid];
    p.out[b * DM + cc * 32 + tid] = __float2bfloat16(v);
  }
  STAMP(15);
  STAMP_FLUSH(14, 16);
}

__global__ void __launch_bounds__(256) combine2(Params p) {
  STAMP_DECL;
  STAMP(14);
  const int b = blockIdx.x >> 4;
  const int tid = threadIdx.x, lane = tid & 63;
  const int col0 = (blockIdx.x & 15) * 32 + (tid >> 6) * 8 + (lane >> 5) * 4;
  const int h = lane & 31;
  const float* ml = p.ml + (size_t)(b * NH + h) * 8;
  const f32x4 mlA = *reinterpret_cast<const f32x4*>(ml);
  const f32x4 mlB = *reinterpret_cast<const f32x4*>(ml + 4);
  const float* y = p.y + (size_t)(b * NH + h) * 4 * DM + col0;
  const f32x4 y0 = *reinterpret_cast<const f32x4*>(y);
  const f32x4 y1 = *reinterpret_cast<const f32x4*>(y + DM);
  const f32x4 y2 = *reinterpret_cast<const f32x4*>(y + 2 * DM);
  const f32x4 y3 = *reinterpret_cast<const f32x4*>(y + 3 * DM);
  const u32x4 xw = reinterpret_cast<const u32x4*>(p.x)[b * (DM / 8) + lane];
  const u32x4 gw = reinterpret_cast<const u32x4*>(p.gate)[lane];
  float gs = 0.f;
#pragma unroll
  for (int k = 0; k < 4; ++k) {
    const unsigned pr = cvtpk(bf_lo(xw[k]) * bf_lo(gw[k]), bf_hi(xw[k]) * bf_hi(gw[k]));
    gs += bf_lo(pr) + bf_hi(pr);
  }
  const float z = rbf(xrow_sum(row_sum(gs)));
  const float g = rbf(1.f / (1.f + __expf(-z)));
  const float omg = rbf(1.f - g);

  const float MA = fmaxf(mlA[0], mlA[2]), MB = fmaxf(mlB[0], mlB[2]);
  const float a0 = __builtin_amdgcn_exp2f(mlA[0] - MA), a1 = __builtin_amdgcn_exp2f(mlA[2] - MA);
  const float b0 = __builtin_amdgcn_exp2f(mlB[0] - MB), b1 = __builtin_amdgcn_exp2f(mlB[2] - MB);
  const float iA = 1.f / (a0 * mlA[1] + a1 * mlA[3]);
  const float iB = 1.f / (b0 * mlB[1] + b1 * mlB[3]);
  const f32x4 sa = (y0 * a0 + y1 * a1) * iA;
  const f32x4 sb = (y2 * b0 + y3 * b1) * iB;
  float u[4];
#pragma unroll
  for (int k = 0; k < 4; ++k) u[k] = tr16(sa[k], sb[k]);
  const int c = lane & 15;
  float v0, v1;
  { const float t1 = u[0] + dpp<XOR8>(u[0]), t2 = u[2] + dpp<XOR8>(u[2]); v0 = (c & 8) ? t2 : t1; }
  { const float t1 = u[1] + dpp<XOR8>(u[1]), t2 = u[3] + dpp<XOR8>(u[3]); v1 = (c & 8) ? t2 : t1; }
  float w;
  { const float t1 = v0 + dpp<XOR7>(v0), t2 = v1 + dpp<XOR7>(v1); w = (c & 4) ? t2 : t1; }
  w += dpp<XOR2>(w);
  w += dpp<XOR1>(w);
  const float t = rbf(rbf(w) * ((lane & 16) ? omg : g));
  const float o = tr16(t, t);
  if ((lane & 19) == 0) p.out[b * DM + col0 + (c >> 2)] = __float2bfloat16(o);
  STAMP(15);
  STAMP_FLUSH(14, 16);
}

}

extern "C" void kernel_launch(void* const* d_in, const int* in_sizes, int n_in, void* d_out, int out_size, void* d_ws,
                              size_t ws_size, hipStream_t stream) {
  Params p;
  p.x = (const uint16_t*)d_in[0];
  p.kA = (const uint16_t*)d_in[1];
  p.vA = (const uint16_t*)d_in[2];
  p.wqA = (const uint16_t*)d_in[3];
  p.woA = (const uint16_t*)d_in[4];
  p.kB = (const uint16_t*)d_in[6];
  p.vB = (const uint16_t*)d_in[7];
  p.wqB = (const uint16_t*)d_in[8];
  p.woB = (const uint16_t*)d_in[9];
  p.gate = (const uint16_t*)d_in[10];
  p.y = (float*)d_ws;
  p.ml = (float*)((char*)d_ws + (size_t)NB * NH * 4 * DM * 4);
  p.out = (__hip_bfloat16*)d_out;
  attn_partial<<<256, NT, 0, stream>>>(p);
  combine2<<<64, 256, 0, stream>>>(p);
}
```

```cpp
#include <hip/hip_runtime.h>
#include <hip/hip_bf16.h>
#include <stdint.h>

#define STAMP(k)
#define STAMP_DECL
#define STAMP_FLUSH(a, b)

namespace {

constexpr int NB = 4, DM = 512, NH = 32, SK = 4096, DH = 128, WIN = 1024;
constexpr int HD = NH * DH;

constexpr int NW = 8;
constexpr int NT = NW * 64;
constexpr int NL = 16;
constexpr int LTOT = 160;
constexpr int R = 4;
constexpr int PWIN = 2;
constexpr int TAILN = 2;
constexpr int DPOST = 8;
constexpr int DRAIN = 2;
constexpr int WO_L0 = 80;
constexpr int THR_L0 = 112, THR_N = 19, THR_N31 = 9;
constexpr float SCALE = 0.08838834764831845f, LOG2E = 1.4426950408889634f;
constexpr unsigned KVBYTES = (unsigned)NB * NH * SK * DH * 2;
constexpr unsigned WBYTES = (unsigned)DM * HD * 2;
constexpr int AUX_NT = 2;

using u32x4 = __attribute__((ext_vector_type(4))) unsigned;
using f32x4 = __attribute__((ext_vector_type(4))) float;
using f32x2 = __attribute__((ext_vector_type(2))) float;
using bf16x2 = __attribute__((ext_vector_type(2))) __bf16;
using rsrc_t = __amdgpu_buffer_rsrc_t;

__device__ __forceinline__ float dot2(unsigned k, unsigned q, float acc) {
  return __builtin_amdgcn_fdot2_f32_bf16(__builtin_bit_cast(bf16x2, k), __builtin_bit_cast(bf16x2, q), acc, false);
}
__device__ __forceinline__ unsigned cvtpk(float lo, float hi) {
  const bf16x2 v = {(__bf16)lo, (__bf16)hi};
  return __builtin_bit_cast(unsigned, v);
}
template <int CTRL>
__device__ __forceinline__ float dpp(float x) {
  return __builtin_bit_cast(float, __builtin_amdgcn_mov_dpp(__builtin_bit_cast(int, x), CTRL, 0xf, 0xf, true));
}
constexpr int XOR1 = 0xB1, XOR2 = 0x4E, XOR7 = 0x141, XOR8 = 0x128;
constexpr int BCAST = 0x150;

__device__ __forceinline__ float bf_lo(unsigned w) { return __builtin_bit_cast(float, w << 16); }
__device__ __forceinline__ float bf_hi(unsigned w) { return __builtin_bit_cast(float, w & 0xffff0000u); }
__device__ __forceinline__ float rbf(float v) { return bf_lo(cvtpk(v, v)); }

__device__ __forceinline__ float tr32(float u, float w) {
  auto t = __builtin_amdgcn_permlane32_swap(__float_as_uint(u), __float_as_uint(w), false, false);
  return __uint_as_float(t[0]) + __uint_as_float(t[1]);
}
__device__ __forceinline__ float tr16(float u, float w) {
  auto t = __builtin_amdgcn_permlane16_swap(__float_as_uint(u), __float_as_uint(w), false, false);
  return __uint_as_float(t[0]) + __uint_as_float(t[1]);
}
__device__ __forceinline__ float xrow_sum(float x) { x = tr16(x, x); return tr32(x, x); }
__device__ __forceinline__ float xrow_max(float x) {
  auto s = __builtin_amdgcn_permlane16_swap(__float_as_uint(x), __float_as_uint(x), false, false);
  x = fmaxf(__uint_as_float(s[0]), __uint_as_float(s[1]));
  auto t = __builtin_amdgcn_permlane32_swap(__float_as_uint(x), __float_as_uint(x), false, false);
  return fmaxf(__uint_as_float(t[0]), __uint_as_float(t[1]));
}
__device__ __forceinline__ float row_max(float x) {
  x = fmaxf(x, dpp<XOR8>(x));
  x = fmaxf(x, dpp<XOR7>(x));
  x = fmaxf(x, dpp<XOR2>(x));
  x = fmaxf(x, dpp<XOR1>(x));
  return x;
}
__device__ __forceinline__ float row_sum(float x) {
  x += dpp<XOR8>(x);
  x += dpp<XOR7>(x);
  x += dpp<XOR2>(x);
  x += dpp<XOR1>(x);
  return x;
}
__device__ __forceinline__ f32x2 rows_reduce8(const float (&a)[8]) {
  const float u0 = tr32(a[0], a[4]), u1 = tr32(a[1], a[5]), u2 = tr32(a[2], a[6]), u3 = tr32(a[3], a[7]);
  return f32x2{tr16(u0, u2), tr16(u1, u3)};
}
#define PIN() __builtin_amdgcn_sched_barrier(0)
template <int CNT, int STEP>
__device__ __forceinline__ void throttle(int n) {
  asm volatile("s_cmp_le_u32 %0, %2\n\ts_cbranch_scc1 1f\n\ts_waitcnt vmcnt(%1)\n1:" ::"s"(n), "i"(CNT), "i"(STEP) : "scc");
}
template <int CNT>
__device__ __forceinline__ void pace() { asm volatile("s_waitcnt vmcnt(%0)" ::"i"(CNT)); }

struct Params {
  const uint16_t* x;
  const uint16_t* kA; const uint16_t* vA; const uint16_t* wqA; const uint16_t* woA;
  const uint16_t* kB; const uint16_t* vB; const uint16_t* wqB; const uint16_t* woB;
  const uint16_t* gate;
  float* y;
  float* ml;
  __hip_bfloat16* out;
};

constexpr int L_QP = 0;
constexpr int REC = 132;
constexpr int L_REC = L_QP + 2 * NW * 128;
constexpr int L_O = L_REC + 2 * NW * REC;
constexpr int L_OB = L_O + 64;
constexpr int L_CNT = L_OB + NW * 64;
constexpr int L_END = L_CNT + 4;

struct Ctx {
  rsrc_t kA, vA, kB, vB;
  unsigned offA, offB;
  unsigned vo;
  int thr;
};

template <int L>
__device__ __forceinline__ u32x4 ld_stream(const Ctx& c) {
  constexpr int F = L / 16, N = L % 16;
  constexpr bool ISB = F < 2, ISV = (F == 1 || F >= 6);
  constexpr unsigned rel = (N / 4) * 32768u + (ISB ? 0u : ((F - 2) & 3) * 131072u);
  const unsigned so = (ISB ? c.offB : c.offA) + rel;
  const rsrc_t& r = ISB ? (ISV ? c.vB : c.kB) : (ISV ? c.vA : c.kA);
  return __builtin_amdgcn_raw_buffer_load_b128(r, c.vo + (N % 4) * 1024, so, AUX_NT);
}
__device__ __forceinline__ u32x4 ld_w(const rsrc_t& r, unsigned vo, unsigned so) {
  return __builtin_amdgcn_raw_buffer_load_b128(r, vo, so, AUX_NT);
}

template <int I>
__device__ __forceinline__ void q_step(float (&qa)[8], const u32x4& w, const u32x4 (&xv)[2]) {
  const unsigned xw = xv[I >> 3][(I >> 1) & 3];
  const float xf = (I & 1) ? bf_hi(xw) : bf_lo(xw);
#pragma unroll
  for (int k = 0; k < 4; ++k) {
    qa[2 * k] = fmaf(xf, bf_lo(w[k]), qa[2 * k]);
    qa[2 * k + 1] = fmaf(xf, bf_hi(w[k]), qa[2 * k + 1]);
  }
}
__device__ __forceinline__ void q_post(const float (&qa)[8], float* lds, int wave, int r, int c) {
  *reinterpret_cast<f32x2*>(lds + wave * 128 + 8 * c + 2 * r) = rows_reduce8(qa);
}
__device__ __forceinline__ u32x4 q_gather(const float* lds, int c) {
  f32x4 a = {0.f, 0.f, 0.f, 0.f}, b = {0.f, 0.f, 0.f, 0.f};
#pragma unroll
  for (int w = 0; w < NW; ++w) {
    a += *reinterpret_cast<const f32x4*>(lds + w * 128 + 8 * c);
    b += *reinterpret_cast<const f32x4*>(lds + w * 128 + 8 * c + 4);
  }
  return u32x4{cvtpk(a[0], a[1]), cvtpk(a[2], a[3]), cvtpk(b[0], b[1]), cvtpk(b[2], b[3])};
}

struct State { float m, l, acc[8]; };

__device__ __forceinline__ float qk(const u32x4& k, const u32x4& q) {
  float d = dot2(k[0], q[0], 0.f);
  d = dot2(k[1], q[1], d);
  d = dot2(k[2], q[2], d);
  d = dot2(k[3], q[3], d);
  asm volatile("" : "+v"(d));
  return d;
}

__device__ __forceinline__ float score(float (&x)[NL], int c) {
#define TR_STEP(HALF, CTRL, BIT)                                          \
  _Pragma("unroll") for (int h = 0; h < (HALF); ++h) {                     \
    const float lo_ = x[h], hi_ = x[h + (HALF)];                           \
    const float t1_ = lo_ + dpp<CTRL>(lo_), t2_ = hi_ + dpp<CTRL>(hi_);    \
    x[h] = (c & (BIT)) ? t2_ : t1_;                                        \
  }
  TR_STEP(8, XOR8, 8) TR_STEP(4, XOR7, 4) TR_STEP(2, XOR2, 2) TR_STEP(1, XOR1, 1)
#undef TR_STEP
  return rbf(rbf(x[0]) * SCALE) * LOG2E;
}

template <int I>
__device__ __forceinline__ void pv_step(State& st, float p, const u32x4& v) {
  const float pb = dpp<BCAST + I>(p);
#pragma unroll
  for (int k = 0; k < 4; ++k) {
    st.acc[2 * k] = fmaf(pb, bf_lo(v[k]), st.acc[2 * k]);
    st.acc[2 * k + 1] = fmaf(pb, bf_hi(v[k]), st.acc[2 * k + 1]);
  }
  asm volatile("" : "+v"(st.acc[0]), "+v"(st.acc[1]), "+v"(st.acc[2]), "+v"(st.acc[3]), "+v"(st.acc[4]), "+v"(st.acc[5]),
               "+v"(st.acc[6]), "+v"(st.acc[7]));
}

__device__ __forceinline__ void wave_record(State& st, float* lds, int wave, int r, int c, int lane) {
  const float M = xrow_max(st.m);
  const float w = __builtin_amdgcn_exp2f(st.m - M);
  const float L = xrow_sum(row_sum(st.l) * w);
  float a[8];
#pragma unroll
  for (int k = 0; k < 8; ++k) a[k] = st.acc[k] * w;
  float* rec = lds + wave * REC;
  *reinterpret_cast<f32x2*>(rec + 8 * c + 2 * r) = rows_reduce8(a);
  if (lane == 0) *reinterpret_cast<f32x2*>(rec + 128) = f32x2{M, L};
}
__device__ __forceinline__ void wg_merge(const float* rec, unsigned* o_out, int t, float* ml_out, bool store_ml) {
  const int d = (t >> 3) + 16 * (t & 7);
  float M = rec[128];
#pragma unroll
  for (int w = 1; w < NW; ++w) M = fmaxf(M, rec[w * REC + 128]);
  float o0 = 0.f, o1 = 0.f, L = 0.f;
#pragma unroll
  for (int w = 0; w < NW; ++w) {
    const float wt = __builtin_amdgcn_exp2f(rec[w * REC + 128] - M);
    o0 = fmaf(wt, rec[w * REC + d], o0);
    o1 = fmaf(wt, rec[w * REC + d + 8], o1);
    L = fmaf(wt, rec[w * REC + 129], L);
  }
  o_out[t] = cvtpk(o0, o1);
  if (t == 0 && store_ml) *reinterpret_cast<float2*>(ml_out) = make_float2(M, L);
}
__device__ __forceinline__ void wo_pair(u32x4 (&W)[NL]) {
#pragma unroll
  for (int j = 0; j < NL / 2; ++j)
#pragma unroll
    for (int k = 0; k < 4; ++k) {
      const unsigned a = W[2 * j][k], b = W[2 * j + 1][k];
      W[2 * j][k] = __builtin_amdgcn_perm(b, a, 0x05040100u);
      W[2 * j + 1][k] = __builtin_amdgcn_perm(b, a, 0x07060302u);
    }
}
__device__ __forceinline__ void out_proj(const u32x4 (&W)[NL], const float* lds, int wave, int lane, float* y_out) {
  const int r8 = lane >> 3, c8 = lane & 7;
  const u32x4 oa = *reinterpret_cast<const u32x4*>(lds + r8 * 8), ob = *reinterpret_cast<const u32x4*>(lds + r8 * 8 + 4);
  float y[8];
#pragma unroll
  for (int k = 0; k < 8; ++k) y[k] = 0.f;
#pragma unroll
  for (int j = 0; j < NL / 2; ++j) {
    const unsigned o = j < 4 ? oa[j & 3] : ob[j & 3];
#pragma unroll
    for (int k = 0; k < 4; ++k) {
      y[2 * k] = dot2(W[2 * j][k], o, y[2 * k]);
      y[2 * k + 1] = dot2(W[2 * j + 1][k], o, y[2 * k + 1]);
    }
  }
  const f32x2 t = rows_reduce8(y);
  const float t0 = t[0] + dpp<XOR8>(t[0]), t1 = t[1] + dpp<XOR8>(t[1]);
  y_out[64 * wave + 8 * c8 + r8] = (lane & 8) ? t1 : t0;
}

__device__ __forceinline__ rsrc_t mk(const void* p, unsigned bytes) {
  return __builtin_amdgcn_make_buffer_rsrc(const_cast<void*>(p), 0, bytes, 0x00020000);
}

__device__ __forceinline__ void lds_arrive(unsigned addr) {
  asm volatile("s_waitcnt lgkmcnt(0)\n\tds_add_u32 %0, %1" ::"v"(addr), "v"(1u) : "memory");
}
template <int N>
__device__ __forceinline__ void lds_wait(unsigned addr) {
  unsigned v, sv;
  asm volatile(
      "1:\n\tds_read_b32 %0, %2\n\ts_waitcnt lgkmcnt(0)\n\tv_readfirstlane_b32 %1, %0\n\ts_nop 1\n\t"
      "s_cmp_ge_u32 %1, %3\n\ts_cbranch_scc1 2f\n\ts_sleep 1\n\ts_branch 1b\n2:"
      : "=&v"(v), "=&s"(sv) : "v"(addr), "i"(N) : "memory", "scc");
}

struct Str {
  const Ctx& c;
  const rsrc_t& rqA; const rsrc_t& rqB; unsigned qvo, qvoB;
  u32x4 S[LTOT], QB[NL], QA[NL];
  template <int P>
  __device__ __forceinline__ void issue() {
    if constexpr (P < 96) {
      constexpr int T = (P % 48) / 3, U = P % 3;
      if constexpr (U == 0) {
        if constexpr (P < 48) QB[T] = ld_w(rqB, qvoB, T * (HD * 2));
        else QA[T] = ld_w(rqA, qvo, T * (HD * 2));
      } else {
        constexpr int L = (P < 48 ? 0 : 32) + 2 * T + (U - 1);
        S[L] = ld_stream<L>(c);
      }
    } else if constexpr (P - 32 < LTOT) {
      S[P - 32] = ld_stream<P - 32>(c);
    }
  }
  template <int P0, int P1>
  __device__ __forceinline__ void range() {
    if constexpr (P0 < P1) { issue<P0>(); range<P0 + 1, P1>(); }
  }
  template <int P0, int P1>
  __device__ __forceinline__ void paced() {
    if constexpr (P0 < P1) { pace<R - 1>(); issue<P0>(); PIN(); paced<P0 + 1, P1>(); }
  }
};
constexpr int p_after(int G) {
  const int full = 3 * (G + PWIN);
  return full <= 96 ? full : 96 + (full - 96) / 3 * TAILN;
}
template <int I0, int I1>
__device__ __forceinline__ void qb_steps(Str& s, float (&qb)[8], const u32x4 (&xv)[2]) {
  if constexpr (I0 < I1) {
    q_step<I0>(qb, s.QB[I0], xv);
    PIN();
    s.template range<p_after(I0), p_after(I0 + 1)>();
    PIN();
    qb_steps<I0 + 1, I1>(s, qb, xv);
  }
}
template <int I0, int I1>
__device__ __forceinline__ void qa_steps(Str& s, float (&qa)[8], const u32x4 (&xv)[2]) {
  if constexpr (I0 < I1) {
    q_step<I0>(qa, s.QA[I0], xv);
    PIN();
    pace<p_after(16 + I0) - (48 + 3 * I0) - 1>();
    s.template range<p_after(16 + I0), p_after(17 + I0)>();
    PIN();
    qa_steps<I0 + 1, I1>(s, qa, xv);
  }
}
template <int L0, int N0, int N1>
__device__ __forceinline__ void pv_steps(Str& s, State& st, float p) {
  if constexpr (N0 < N1) { pv_step<N0>(st, p, s.S[L0 + N0]); pv_steps<L0, N0 + 1, N1>(s, st, p); }
}

constexpr int I_START = p_after(32) - 32 + DPOST;
constexpr int issued_at(int L) {
  const int fast = I_START + (L - 32) / 4;
  const int lim = L + R;
  const int v = fast > lim ? fast : lim;
  if (L >= LTOT - 16) { const int d = LTOT - 16 + R + DRAIN * (L - (LTOT - 16)); return d > LTOT ? LTOT : d; }
  return v > LTOT ? LTOT : v;
}
template <int L>
__device__ __forceinline__ void refill(Str& s, u32x4 (&WA)[NL], u32x4 (&WB)[NL], const rsrc_t& roA, const rsrc_t& roB,
                                       unsigned ovo, unsigned ovoB) {
  constexpr int A = issued_at(L), B = issued_at(L + 1);
  PIN();
  if constexpr (B > A) {
    if constexpr (A > L + R) pace<R - 1>();
    else if constexpr (L >= THR_L0 && L < THR_L0 + THR_N && L < LTOT - 16) throttle<R - 2, L - THR_L0>(s.c.thr);
    s.template range<A + 32, B + 32>();
  }
  if constexpr (L >= WO_L0 && L < WO_L0 + 64 && (L & 1)) {
    constexpr int w = (L - WO_L0) / 2;
    if constexpr (w < 16) WB[w] = ld_w(roB, ovoB, w * 8 * DM * 2);
    else WA[w - 16] = ld_w(roA, ovo, (w - 16) * 8 * DM * 2);
  }
  PIN();
}
template <int F, int N>
__device__ __forceinline__ void k_loop(Str& s, const u32x4& q, float (&x)[NL], u32x4 (&WA)[NL], u32x4 (&WB)[NL], const rsrc_t& roA,
                                       const rsrc_t& roB, unsigned ovo, unsigned ovoB) {
  if constexpr (N < NL) {
    x[N] = qk(s.S[F * 16 + N], q);
    refill<F * 16 + N>(s, WA, WB, roA, roB, ovo, ovoB);
    k_loop<F, N + 1>(s, q, x, WA, WB, roA, roB, ovo, ovoB);
  }
}
template <int F, int N, int N1 = NL>
__device__ __forceinline__ void v_loop(Str& s, State& st, float p, u32x4 (&WA)[NL], u32x4 (&WB)[NL], const rsrc_t& roA,
                                       const rsrc_t& roB, unsigned ovo, unsigned ovoB) {
  if constexpr (N < N1) {
    asm volatile("" ::"v"(s.S[F * 16 + N]));
    refill<F * 16 + N>(s, WA, WB, roA, roB, ovo, ovoB);
    pv_step<N>(st, p, s.S[F * 16 + N]);
    PIN();
    v_loop<F, N + 1, N1>(s, st, p, WA, WB, roA, roB, ovo, ovoB);
  }
}

__device__ __forceinline__ void reset(State& st) {
  st.m = -INFINITY; st.l = 0.f;
#pragma unroll
  for (int k = 0; k < 8; ++k) st.acc[k] = 0.f;
}

__global__ void __launch_bounds__(NT, 2) attn_partial(Params p) {
  __shared__ __attribute__((aligned(16))) float lds[L_END];
  const int bid = blockIdx.x;
  STAMP_DECL;
  STAMP(0);
  const int h = (bid & 7) * 4 + ((bid >> 3) & 3);
  const int hB = h ^ 1;
  const int j = bid >> 5;
  const int b = j >> 1, half = j & 1;
  const int tid = threadIdx.x, lane = tid & 63;
  const int wave = __builtin_amdgcn_readfirstlane(tid >> 6);
  const int r = lane >> 4, c = lane & 15;

  Ctx cx;
  cx.kA = mk(p.kA, KVBYTES); cx.vA = mk(p.vA, KVBYTES); cx.kB = mk(p.kB, KVBYTES); cx.vB = mk(p.vB, KVBYTES);
  cx.offB = ((unsigned)(b * NH + hB) * SK + (SK - WIN) + half * 512) * (DH * 2) + wave * 4096;
  cx.offA = ((unsigned)(b * NH + h) * SK + half * 2048) * (DH * 2) + wave * 4096;
  cx.vo = lane * 16;
  cx.thr = __builtin_amdgcn_readfirstlane(((h & 3) == 1 || (hB & 3) == 1) ? 0 : ((h | 1) == 31 ? THR_N31 : THR_N));
  const rsrc_t rqA = mk(p.wqA, WBYTES), rqB = mk(p.wqB, WBYTES), roA = mk(p.woA, WBYTES), roB = mk(p.woB, WBYTES);
  const unsigned qvo = ((64 * wave + 16 * r) * HD + h * DH + 8 * c) * 2;
  const unsigned qvoB = ((64 * wave + 16 * r) * HD + hB * DH + 8 * c) * 2;
  const unsigned ovo = ((h * DH + (lane >> 3)) * DM + 64 * wave + 8 * (lane & 7)) * 2;
  const unsigned ovoB = ((hB * DH + (lane >> 3)) * DM + 64 * wave + 8 * (lane & 7)) * 2;

  u32x4 WA[NL], WB[NL], xv[2];
  {
    const char* xp = reinterpret_cast<const char*>(p.x) + (b * DM + 64 * wave + 16 * r) * 2;
    xv[0] = *reinterpret_cast<const u32x4*>(xp);
    xv[1] = *reinterpret_cast<const u32x4*>(xp + 16);
  }
  unsigned* cnt = reinterpret_cast<unsigned*>(lds) + L_CNT;
  const unsigned cnt_addr = (unsigned)(size_t)(__attribute__((address_space(3))) void*)cnt;
  if (tid == 0) *cnt = 0u;
  Str s{cx, rqA, rqB, qvo, qvoB};
  PIN();
  s.range<0, p_after(0)>();
  PIN();
  STAMP(1);

  float* yb = p.y + (size_t)((b * NH + h) * 4 + half) * DM;
  float* mlb = p.ml + (size_t)((b * NH + h) * 4 + half) * 2;
  float* ybB = p.y + (size_t)((b * NH + hB) * 4 + 2 + half) * DM;
  float* mlbB = p.ml + (size_t)((b * NH + hB) * 4 + 2 + half) * 2;

  float qa[8];
#pragma unroll
  for (int k = 0; k < 8; ++k) qa[k] = 0.f;
  qb_steps<0, NL>(s, qa, xv);
  q_post(qa, lds + L_QP, wave, r, c);
  __syncthreads();
  STAMP(2);

  u32x4 q = q_gather(lds + L_QP, c);
  State st;
  float x[NL], pr;
  reset(st);
#pragma unroll
  for (int k = 0; k < 8; ++k) qa[k] = 0.f;
#pragma unroll
  for (int i = 0; i < NL; ++i) x[i] = qk(s.S[i], q);
  PIN();
  qa_steps<0, 1>(s, qa, xv);
  {
    const float s0 = score(x, c);
    st.m = row_max(s0);
    pr = __builtin_amdgcn_exp2f(s0 - st.m);
    st.l = pr;
  }
  PIN();
  STAMP(3);
  qa_steps<1, 2>(s, qa, xv);
  pv_steps<16, 0, 4>(s, st, pr);   PIN(); qa_steps<2, 3>(s, qa, xv);
  pv_steps<16, 4, 8>(s, st, pr);   PIN(); qa_steps<3, 4>(s, qa, xv);
  pv_steps<16, 8, 12>(s, st, pr);  PIN(); qa_steps<4, 5>(s, qa, xv);
  pv_steps<16, 12, 16>(s, st, pr); PIN(); qa_steps<5, 6>(s, qa, xv);
  wave_record(st, lds + L_REC + NW * REC, wave, r, c, lane);
  reset(st);
  PIN();
  STAMP(4);
  qa_steps<6, NL>(s, qa, xv);
  q_post(qa, lds + L_QP + NW * 128, wave, r, c);
  lds_arrive(cnt_addr);
  PIN();
  s.paced<p_after(32), p_after(32) + DPOST>();
  lds_wait<64 * NW>(cnt_addr);
  STAMP(5);
  q = q_gather(lds + L_QP + NW * 128, c);
  PIN();

  float sc[4];
  k_loop<2, 0>(s, q, x, WA, WB, roA, roB, ovo, ovoB);
  sc[0] = score(x, c);
  k_loop<3, 0>(s, q, x, WA, WB, roA, roB, ovo, ovoB);
  sc[1] = score(x, c);
  STAMP(6);
  k_loop<4, 0>(s, q, x, WA, WB, roA, roB, ovo, ovoB);
  sc[2] = score(x, c);
  wg_merge(lds + L_REC + NW * REC, reinterpret_cast<unsigned*>(lds) + L_OB + wave * 64, lane, mlbB, wave == 0);
  STAMP(7);
  k_loop<5, 0>(s, q, x, WA, WB, roA, roB, ovo, ovoB);
  sc[3] = score(x, c);
  st.m = row_max(fmaxf(fmaxf(sc[0], sc[1]), fmaxf(sc[2], sc[3])));
#pragma unroll
  for (int i = 0; i < 4; ++i) sc[i] = __builtin_amdgcn_exp2f(sc[i] - st.m);
  st.l = (sc[0] + sc[1]) + (sc[2] + sc[3]);
  STAMP(8);
  v_loop<6, 0>(s, st, sc[0], WA, WB, roA, roB, ovo, ovoB);
  v_loop<7, 0>(s, st, sc[1], WA, WB, roA, roB, ovo, ovoB);
  wo_pair(WB);
  out_proj(WB, lds + L_OB + wave * 64, wave, lane, ybB);
  STAMP(9);
  v_loop<8, 0>(s, st, sc[2], WA, WB, roA, roB, ovo, ovoB);
  v_loop<9, 0, 7>(s, st, sc[3], WA, WB, roA, roB, ovo, ovoB);
  wo_pair(WA);
#pragma unroll
  for (int i = 0; i < NL; ++i) asm volatile("" : "+v"(WA[i]));
  PIN();
  v_loop<9, 7>(s, st, sc[3], WA, WB, roA, roB, ovo, ovoB);
  STAMP(10);

  wave_record(st, lds + L_REC, wave, r, c, lane);
  __syncthreads();
  STAMP(11);
  if (wave == 0) wg_merge(lds + L_REC, reinterpret_cast<unsigned*>(lds) + L_O, lane, mlb, true);
  __syncthreads();
  STAMP(12);
  out_proj(WA, lds + L_O, wave, lane, yb);
  STAMP(13);
  STAMP_FLUSH(0, 14);
}

__global__ void __launch_bounds__(256) combine(Params p) {
  STAMP_DECL;
  STAMP(14);
  const int b = blockIdx.x >> 4;
  const int tid = threadIdx.x, lane = tid & 63;
  const int col0 = (blockIdx.x & 15) * 32 + (tid >> 6) * 8 + (lane >> 5) * 4;
  const int h = lane & 31;
  const float* ml = p.ml + (size_t)(b * NH + h) * 8;
  const f32x4 mlA = *reinterpret_cast<const f32x4*>(ml);
  const f32x4 mlB = *reinterpret_cast<const f32x4*>(ml + 4);
  const float* y = p.y + (size_t)(b * NH + h) * 4 * DM + col0;
  const f32x4 y0 = *reinterpret_cast<const f32x4*>(y);
  const f32x4 y1 = *reinterpret_cast<const f32x4*>(y + DM);
  const f32x4 y2 = *reinterpret_cast<const f32x4*>(y + 2 * DM);
  const f32x4 y3 = *reinterpret_cast<const f32x4*>(y + 3 * DM);
  const u32x4 xw = reinterpret_cast<const u32x4*>(p.x)[b * (DM / 8) + lane];
  const u32x4 gw = reinterpret_cast<const u32x4*>(p.gate)[lane];
  float gs = 0.f;
#pragma unroll
  for (int k = 0; k < 4; ++k) {
    const unsigned pr = cvtpk(bf_lo(xw[k]) * bf_lo(gw[k]), bf_hi(xw[k]) * bf_hi(gw[k]));
    gs += bf_lo(pr) + bf_hi(pr);
  }
  const float z = rbf(xrow_sum(row_sum(gs)));
  const float g = rbf(1.f / (1.f + __expf(-z)));
  const float omg = rbf(1.f - g);

  const float MA = fmaxf(mlA[0], mlA[2]), MB = fmaxf(mlB[0], mlB[2]);
  const float a0 = __builtin_amdgcn_exp2f(mlA[0] - MA), a1 = __builtin_amdgcn_exp2f(mlA[2] - MA);
  const float b0 = __builtin_amdgcn_exp2f(mlB[0] - MB), b1 = __builtin_amdgcn_exp2f(mlB[2] - MB);
  const float iA = 1.f / (a0 * mlA[1] + a1 * mlA[3]);
  const float iB = 1.f / (b0 * mlB[1] + b1 * mlB[3]);
  const f32x4 sa = (y0 * a0 + y1 * a1) * iA;
  const f32x4 sb = (y2 * b0 + y3 * b1) * iB;
  float u[4];
#pragma unroll
  for (int k = 0; k < 4; ++k) u[k] = tr16(sa[k], sb[k]);
  const int c = lane & 15;
  float v0, v1;
  { const float t1 = u[0] + dpp<XOR8>(u[0]), t2 = u[2] + dpp<XOR8>(u[2]); v0 = (c & 8) ? t2 : t1; }
  { const float t1 = u[1] + dpp<XOR8>(u[1]), t2 = u[3] + dpp<XOR8>(u[3]); v1 = (c & 8) ? t2 : t1; }
  float w;
  { const float t1 = v0 + dpp<XOR7>(v0), t2 = v1 + dpp<XOR7>(v1); w = (c & 4) ? t2 : t1; }
  w += dpp<XOR2>(w);
  w += dpp<XOR1>(w);
  const float t = rbf(rbf(w) * ((lane & 16) ? omg : g));
  const float o = tr16(t, t);
  if ((lane & 19) == 0) p.out[b * DM + col0 + (c >> 2)] = __float2bfloat16(o);
  STAMP(15);
  STAMP_FLUSH(14, 16);
}

}

extern "C" void kernel_launch(void* const* d_in, const int* in_sizes, int n_in, void* d_out, int out_size, void* d_ws,
                              size_t ws_size, hipStream_t stream) {
  Params p;
  p.x = (const uint16_t*)d_in[0];
  p.kA = (const uint16_t*)d_in[1];
  p.vA = (const uint16_t*)d_in[2];
  p.wqA = (const uint16_t*)d_in[3];
  p.woA = (const uint16_t*)d_in[4];
  p.kB = (const uint16_t*)d_in[6];
  p.vB = (const uint16_t*)d_in[7];
  p.wqB = (const uint16_t*)d_in[8];
  p.woB = (const uint16_t*)d_in[9];
  p.gate = (const uint16_t*)d_in[10];
  p.y = (float*)d_ws;
  p.ml = (float*)((char*)d_ws + (size_t)NB * NH * 4 * DM * 4);
  p.out = (__hip_bfloat16*)d_out;
  attn_partial<<<256, NT, 0, stream>>>(p);
  combine<<<64, 256, 0, stream>>>(p);
}
```

```cpp
#include <hip/hip_runtime.h>
#include <hip/hip_bf16.h>
#include <stdint.h>

namespace {

constexpr int NB = 4, DM = 512, NH = 32, SK = 4096, DH = 128, WIN = 1024;
constexpr int HD = NH * DH;

constexpr int NW = 8;
constexpr int NT = NW * 64;
constexpr int NL = 16;
constexpr int LTOT = 160;
constexpr int R = 4;
constexpr int PWIN = 2;
constexpr int TAILN = 2;
constexpr int DPOST = 8;
constexpr int DRAIN = 2;
constexpr int WO_L0 = 80;
constexpr int THR_L0 = 112, THR_N = 19, THR_N31 = 9;
constexpr float SCALE = 0.08838834764831845f, LOG2E = 1.4426950408889634f;
constexpr unsigned KVBYTES = (unsigned)NB * NH * SK * DH * 2;
constexpr unsigned WBYTES = (unsigned)DM * HD * 2;
constexpr int AUX_NT = 2;

using u32x4 = __attribute__((ext_vector_type(4))) unsigned;
using f32x4 = __attribute__((ext_vector_type(4))) float;
using f32x2 = __attribute__((ext_vector_type(2))) float;
using bf16x2 = __attribute__((ext_vector_type(2))) __bf16;
using rsrc_t = __amdgpu_buffer_rsrc_t;

__device__ __forceinline__ float dot2(unsigned k, unsigned q, float acc) {
  return __builtin_amdgcn_fdot2_f32_bf16(__builtin_bit_cast(bf16x2, k), __builtin_bit_cast(bf16x2, q), acc, false);
}
__device__ __forceinline__ unsigned cvtpk(float lo, float hi) {
  const bf16x2 v = {(__bf16)lo, (__bf16)hi};
  return __builtin_bit_cast(unsigned, v);
}
template <int CTRL>
__device__ __forceinline__ float dpp(float x) {
  return __builtin_bit_cast(float, __builtin_amdgcn_mov_dpp(__builtin_bit_cast(int, x), CTRL, 0xf, 0xf, true));
}
constexpr int XOR1 = 0xB1, XOR2 = 0x4E, XOR7 = 0x141, XOR8 = 0x128;
constexpr int BCAST = 0x150;

__device__ __forceinline__ float bf_lo(unsigned w) { return __builtin_bit_cast(float, w << 16); }
__device__ __forceinline__ float bf_hi(unsigned w) { return __builtin_bit_cast(float, w & 0xffff0000u); }
__device__ __forceinline__ float rbf(float v) { return bf_lo(cvtpk(v, v)); }

__device__ __forceinline__ float tr32(float u, float w) {
  auto t = __builtin_amdgcn_permlane32_swap(__float_as_uint(u), __float_as_uint(w), false, false);
  return __uint_as_float(t[0]) + __uint_as_float(t[1]);
}
__device__ __forceinline__ float tr16(float u, float w) {
  auto t = __builtin_amdgcn_permlane16_swap(__float_as_uint(u), __float_as_uint(w), false, false);
  return __uint_as_float(t[0]) + __uint_as_float(t[1]);
}
__device__ __forceinline__ float xrow_sum(float x) { x = tr16(x, x); return tr32(x, x); }
__device__ __forceinline__ float xrow_max(float x) {
  auto s = __builtin_amdgcn_permlane16_swap(__float_as_uint(x), __float_as_uint(x), false, false);
  x = fmaxf(__uint_as_float(s[0]), __uint_as_float(s[1]));
  auto t = __builtin_amdgcn_permlane32_swap(__float_as_uint(x), __float_as_uint(x), false, false);
  return fmaxf(__uint_as_float(t[0]), __uint_as_float(t[1]));
}
__device__ __forceinline__ float row_max(float x) {
  x = fmaxf(x, dpp<XOR8>(x));
  x = fmaxf(x, dpp<XOR7>(x));
  x = fmaxf(x, dpp<XOR2>(x));
  x = fmaxf(x, dpp<XOR1>(x));
  return x;
}
__device__ __forceinline__ float row_sum(float x) {
  x += dpp<XOR8>(x);
  x += dpp<XOR7>(x);
  x += dpp<XOR2>(x);
  x += dpp<XOR1>(x);
  return x;
}
__device__ __forceinline__ f32x2 rows_reduce8(const float (&a)[8]) {
  const float u0 = tr32(a[0], a[4]), u1 = tr32(a[1], a[5]), u2 = tr32(a[2], a[6]), u3 = tr32(a[3], a[7]);
  return f32x2{tr16(u0, u2), tr16(u1, u3)};
}
#define PIN() __builtin_amdgcn_sched_barrier(0)
template <int CNT, int STEP>
__device__ __forceinline__ void throttle(int n) {
  asm volatile("s_cmp_le_u32 %0, %2\n\ts_cbranch_scc1 1f\n\ts_waitcnt vmcnt(%1)\n1:" ::"s"(n), "i"(CNT), "i"(STEP) : "scc");
}
template <int CNT>
__device__ __forceinline__ void pace() { asm volatile("s_waitcnt vmcnt(%0)" ::"i"(CNT)); }

struct Params {
  const uint16_t* x;
  const uint16_t* kA; const uint16_t* vA; const uint16_t* wqA; const uint16_t* woA;
  const uint16_t* kB; const uint16_t* vB; const uint16_t* wqB; const uint16_t* woB;
  const uint16_t* gate;
  float* y;
  float* ml;
  __hip_bfloat16* out;
};

constexpr int L_QP = 0;
constexpr int REC = 132;
constexpr int L_REC = L_QP + 2 * NW * 128;
constexpr int L_O = L_REC + 2 * NW * REC;
constexpr int L_OB = L_O + 64;
constexpr int L_CNT = L_OB + NW * 64;
constexpr int L_END = L_CNT + 4;

struct Ctx {
  rsrc_t kA, vA, kB, vB;
  unsigned offA, offB;
  unsigned vo;
  int thr;
};

template <int L>
__device__ __forceinline__ u32x4 ld_stream(const Ctx& c) {
  constexpr int F = L / 16, N = L % 16;
  constexpr bool ISB = F < 2, ISV = (F == 1 || F >= 6);
  constexpr unsigned rel = (N / 4) * 32768u + (ISB ? 0u : ((F - 2) & 3) * 131072u);
  const unsigned so = (ISB ? c.offB : c.offA) + rel;
  const rsrc_t& r = ISB ? (ISV ? c.vB : c.kB) : (ISV ? c.vA : c.kA);
  return __builtin_amdgcn_raw_buffer_load_b128(r, c.vo + (N % 4) * 1024, so, AUX_NT);
}
__device__ __forceinline__ u32x4 ld_w(const rsrc_t& r, unsigned vo, unsigned so) {
  return __builtin_amdgcn_raw_buffer_load_b128(r, vo, so, AUX_NT);
}

template <int I>
__device__ __forceinline__ void q_step(float (&qa)[8], const u32x4& w, const u32x4 (&xv)[2]) {
  const unsigned xw = xv[I >> 3][(I >> 1) & 3];
  const float xf = (I & 1) ? bf_hi(xw) : bf_lo(xw);
#pragma unroll
  for (int k = 0; k < 4; ++k) {
    qa[2 * k] = fmaf(xf, bf_lo(w[k]), qa[2 * k]);
    qa[2 * k + 1] = fmaf(xf, bf_hi(w[k]), qa[2 * k + 1]);
  }
}
__device__ __forceinline__ void q_post(const float (&qa)[8], float* lds, int wave, int r, int c) {
  *reinterpret_cast<f32x2*>(lds + wave * 128 + 8 * c + 2 * r) = rows_reduce8(qa);
}
__device__ __forceinline__ u32x4 q_gather(const float* lds, int c) {
  f32x4 a = {0.f, 0.f, 0.f, 0.f}, b = {0.f, 0.f, 0.f, 0.f};
#pragma unroll
  for (int w = 0; w < NW; ++w) {
    a += *reinterpret_cast<const f32x4*>(lds + w * 128 + 8 * c);
    b += *reinterpret_cast<const f32x4*>(lds + w * 128 + 8 * c + 4);
  }
  return u32x4{cvtpk(a[0], a[1]), cvtpk(a[2], a[3]), cvtpk(b[0], b[1]), cvtpk(b[2], b[3])};
}

struct State { float m, l, acc[8]; };

__device__ __forceinline__ float qk(const u32x4& k, const u32x4& q) {
  float d = dot2(k[0], q[0], 0.f);
  d = dot2(k[1], q[1], d);
  d = dot2(k[2], q[2], d);
  d = dot2(k[3], q[3], d);
  asm volatile("" : "+v"(d));
  return d;
}

__device__ __forceinline__ float score(float (&x)[NL], int c) {
#define TR_STEP(HALF, CTRL, BIT)                                          \
  _Pragma("unroll") for (int h = 0; h < (HALF); ++h) {                     \
    const float lo_ = x[h], hi_ = x[h + (HALF)];                           \
    const float t1_ = lo_ + dpp<CTRL>(lo_), t2_ = hi_ + dpp<CTRL>(hi_);    \
    x[h] = (c & (BIT)) ? t2_ : t1_;                                        \
  }
  TR_STEP(8, XOR8, 8) TR_STEP(4, XOR7, 4) TR_STEP(2, XOR2, 2) TR_STEP(1, XOR1, 1)
#undef TR_STEP
  return rbf(rbf(x[0]) * SCALE) * LOG2E;
}

template <int I>
__device__ __forceinline__ void pv_step(State& st, float p, const u32x4& v) {
  const float pb = dpp<BCAST + I>(p);
#pragma unroll
  for (int k = 0; k < 4; ++k) {
    st.acc[2 * k] = fmaf(pb, bf_lo(v[k]), st.acc[2 * k]);
    st.acc[2 * k + 1] = fmaf(pb, bf_hi(v[k]), st.acc[2 * k + 1]);
  }
  asm volatile("" : "+v"(st.acc[0]), "+v"(st.acc[1]), "+v"(st.acc[2]), "+v"(st.acc[3]), "+v"(st.acc[4]), "+v"(st.acc[5]),
               "+v"(st.acc[6]), "+v"(st.acc[7]));
}

__device__ __forceinline__ void wave_record(State& st, float* lds, int wave, int r, int c, int lane) {
  const float M = xrow_max(st.m);
  const float w = __builtin_amdgcn_exp2f(st.m - M);
  const float L = xrow_sum(row_sum(st.l) * w);
  float a[8];
#pragma unroll
  for (int k = 0; k < 8; ++k) a[k] = st.acc[k] * w;
  float* rec = lds + wave * REC;
  *reinterpret_cast<f32x2*>(rec + 8 * c + 2 * r) = rows_reduce8(a);
  if (lane == 0) *reinterpret_cast<f32x2*>(rec + 128) = f32x2{M, L};
}
__device__ __forceinline__ void wg_merge(const float* rec, unsigned* o_out, int t, float* ml_out, bool store_ml) {
  const int d = (t >> 3) + 16 * (t & 7);
  float M = rec[128];
#pragma unroll
  for (int w = 1; w < NW; ++w) M = fmaxf(M, rec[w * REC + 128]);
  float o0 = 0.f, o1 = 0.f, L = 0.f;
#pragma unroll
  for (int w = 0; w < NW; ++w) {
    const float wt = __builtin_amdgcn_exp2f(rec[w * REC + 128] - M);
    o0 = fmaf(wt, rec[w * REC + d], o0);
    o1 = fmaf(wt, rec[w * REC + d + 8], o1);
    L = fmaf(wt, rec[w * REC + 129], L);
  }
  o_out[t] = cvtpk(o0, o1);
  if (t == 0 && store_ml) *reinterpret_cast<float2*>(ml_out) = make_float2(M, L);
}
__device__ __forceinline__ void wo_pair(u32x4 (&W)[NL]) {
#pragma unroll
  for (int j = 0; j < NL / 2; ++j)
#pragma unroll
    for (int k = 0; k < 4; ++k) {
      const unsigned a = W[2 * j][k], b = W[2 * j + 1][k];
      W[2 * j][k] = __builtin_amdgcn_perm(b, a, 0x05040100u);
      W[2 * j + 1][k] = __builtin_amdgcn_perm(b, a, 0x07060302u);
    }
}
__device__ __forceinline__ void out_proj(const u32x4 (&W)[NL], const float* lds, int wave, int lane, float* y_out) {
  const int r8 = lane >> 3, c8 = lane & 7;
  const u32x4 oa = *reinterpret_cast<const u32x4*>(lds + r8 * 8), ob = *reinterpret_cast<const u32x4*>(lds + r8 * 8 + 4);
  float y[8];
#pragma unroll
  for (int k = 0; k < 8; ++k) y[k] = 0.f;
#pragma unroll
  for (int j = 0; j < NL / 2; ++j) {
    const unsigned o = j < 4 ? oa[j & 3] : ob[j & 3];
#pragma unroll
    for (int k = 0; k < 4; ++k) {
      y[2 * k] = dot2(W[2 * j][k], o, y[2 * k]);
      y[2 * k + 1] = dot2(W[2 * j + 1][k], o, y[2 * k + 1]);
    }
  }
  const f32x2 t = rows_reduce8(y);
  const float t0 = t[0] + dpp<XOR8>(t[0]), t1 = t[1] + dpp<XOR8>(t[1]);
  y_out[64 * wave + 8 * c8 + r8] = (lane & 8) ? t1 : t0;
}

__device__ __forceinline__ rsrc_t mk(const void* p, unsigned bytes) {
  return __builtin_amdgcn_make_buffer_rsrc(const_cast<void*>(p), 0, bytes, 0x00020000);
}

__device__ __forceinline__ void lds_arrive(unsigned addr) {
  asm volatile("s_waitcnt lgkmcnt(0)\n\tds_add_u32 %0, %1" ::"v"(addr), "v"(1u) : "memory");
}
template <int N>
__device__ __forceinline__ void lds_wait(unsigned addr) {
  unsigned v, sv;
  asm volatile(
      "1:\n\tds_read_b32 %0, %2\n\ts_waitcnt lgkmcnt(0)\n\tv_readfirstlane_b32 %1, %0\n\ts_nop 1\n\t"
      "s_cmp_ge_u32 %1, %3\n\ts_cbranch_scc1 2f\n\ts_sleep 1\n\ts_branch 1b\n2:"
      : "=&v"(v), "=&s"(sv) : "v"(addr), "i"(N) : "memory", "scc");
}

struct Str {
  const Ctx& c;
  const rsrc_t& rqA; const rsrc_t& rqB; unsigned qvo, qvoB;
  u32x4 S[LTOT], QB[NL], QA[NL];
  template <int P>
  __device__ __forceinline__ void issue() {
    if constexpr (P < 96) {
      constexpr int T = (P % 48) / 3, U = P % 3;
      if constexpr (U == 0) {
        if constexpr (P < 48) QB[T] = ld_w(rqB, qvoB, T * (HD * 2));
        else QA[T] = ld_w(rqA, qvo, T * (HD * 2));
      } else {
        constexpr int L = (P < 48 ? 0 : 32) + 2 * T + (U - 1);
        S[L] = ld_stream<L>(c);
      }
    } else if constexpr (P - 32 < LTOT) {
      S[P - 32] = ld_stream<P - 32>(c);
    }
  }
  template <int P0, int P1>
  __device__ __forceinline__ void range() {
    if constexpr (P0 < P1) { issue<P0>(); range<P0 + 1, P1>(); }
  }
  template <int P0, int P1>
  __device__ __forceinline__ void paced() {
    if constexpr (P0 < P1) { pace<R - 1>(); issue<P0>(); PIN(); paced<P0 + 1, P1>(); }
  }
};
constexpr int p_after(int G) {
  const int full = 3 * (G + PWIN);
  return full <= 96 ? full : 96 + (full - 96) / 3 * TAILN;
}
template <int I0, int I1>
__device__ __forceinline__ void qb_steps(Str& s, float (&qb)[8], const u32x4 (&xv)[2]) {
  if constexpr (I0 < I1) {
    q_step<I0>(qb, s.QB[I0], xv);
    PIN();
    s.template range<p_after(I0), p_after(I0 + 1)>();
    PIN();
    qb_steps<I0 + 1, I1>(s, qb, xv);
  }
}
template <int I0, int I1>
__device__ __forceinline__ void qa_steps(Str& s, float (&qa)[8], const u32x4 (&xv)[2]) {
  if constexpr (I0 < I1) {
    q_step<I0>(qa, s.QA[I0], xv);
    PIN();
    pace<p_after(16 + I0) - (48 + 3 * I0) - 1>();
    s.template range<p_after(16 + I0), p_after(17 + I0)>();
    PIN();
    qa_steps<I0 + 1, I1>(s, qa, xv);
  }
}
template <int L0, int N0, int N1>
__device__ __forceinline__ void pv_steps(Str& s, State& st, float p) {
  if constexpr (N0 < N1) { pv_step<N0>(st, p, s.S[L0 + N0]); pv_steps<L0, N0 + 1, N1>(s, st, p); }
}

constexpr int I_START = p_after(32) - 32 + DPOST;
constexpr int issued_at(int L) {
  const int fast = I_START + (L - 32) / 4;
  const int lim = L + R;
  const int v = fast > lim ? fast : lim;
  if (L >= LTOT - 16) { const int d = LTOT - 16 + R + DRAIN * (L - (LTOT - 16)); return d > LTOT ? LTOT : d; }
  return v > LTOT ? LTOT : v;
}
template <int L>
__device__ __forceinline__ void refill(Str& s, u32x4 (&WA)[NL], u32x4 (&WB)[NL], const rsrc_t& roA, const rsrc_t& roB,
                                       unsigned ovo, unsigned ovoB) {
  constexpr int A = issued_at(L), B = issued_at(L + 1);
  PIN();
  if constexpr (B > A) {
    if constexpr (A > L + R) pace<R - 1>();
    else if constexpr (L >= THR_L0 && L < THR_L0 + THR_N && L < LTOT - 16) throttle<R - 2, L - THR_L0>(s.c.thr);
    s.template range<A + 32, B + 32>();
  }
  if constexpr (L >= WO_L0 && L < WO_L0 + 64 && (L & 1)) {
    constexpr int w = (L - WO_L0) / 2;
    if constexpr (w < 16) WB[w] = ld_w(roB, ovoB, w * 8 * DM * 2);
    else WA[w - 16] = ld_w(roA, ovo, (w - 16) * 8 * DM * 2);
  }
  PIN();
}
template <int F, int N>
__device__ __forceinline__ void k_loop(Str& s, const u32x4& q, float (&x)[NL], u32x4 (&WA)[NL], u32x4 (&WB)[NL], const rsrc_t& roA,
                                       const rsrc_t& roB, unsigned ovo, unsigned ovoB) {
  if constexpr (N < NL) {
    x[N] = qk(s.S[F * 16 + N], q);
    refill<F * 16 + N>(s, WA, WB, roA, roB, ovo, ovoB);
    k_loop<F, N + 1>(s, q, x, WA, WB, roA, roB, ovo, ovoB);
  }
}
template <int F, int N, int N1 = NL>
__device__ __forceinline__ void v_loop(Str& s, State& st, float p, u32x4 (&WA)[NL], u32x4 (&WB)[NL], const rsrc_t& roA,
                                       const rsrc_t& roB, unsigned ovo, unsigned ovoB) {
  if constexpr (N < N1) {
    asm volatile("" ::"v"(s.S[F * 16 + N]));
    refill<F * 16 + N>(s, WA, WB, roA, roB, ovo, ovoB);
    pv_step<N>(st, p, s.S[F * 16 + N]);
    PIN();
    v_loop<F, N + 1, N1>(s, st, p, WA, WB, roA, roB, ovo, ovoB);
  }
}

__device__ __forceinline__ void reset(State& st) {
  st.m = -INFINITY; st.l = 0.f;
#pragma unroll
  for (int k = 0; k < 8; ++k) st.acc[k] = 0.f;
}

__global__ void __launch_bounds__(NT, 2) attn_partial(Params p) {
  __shared__ __attribute__((aligned(16))) float lds[L_END];
  const int bid = blockIdx.x;
  const int h = (bid & 7) * 4 + ((bid >> 3) & 3);
  const int hB = h ^ 1;
  const int j = bid >> 5;
  const int b = j >> 1, half = j & 1;
  const int tid = threadIdx.x, lane = tid & 63;
  const int wave = __builtin_amdgcn_readfirstlane(tid >> 6);
  const int r = lane >> 4, c = lane & 15;

  Ctx cx;
  cx.kA = mk(p.kA, KVBYTES); cx.vA = mk(p.vA, KVBYTES); cx.kB = mk(p.kB, KVBYTES); cx.vB = mk(p.vB, KVBYTES);
  cx.offB = ((unsigned)(b * NH + hB) * SK + (SK - WIN) + half * 512) * (DH * 2) + wave * 4096;
  cx.offA = ((unsigned)(b * NH + h) * SK + half * 2048) * (DH * 2) + wave * 4096;
  cx.vo = lane * 16;
  cx.thr = __builtin_amdgcn_readfirstlane(((h & 3) == 1 || (hB & 3) == 1) ? 0 : ((h | 1) == 31 ? THR_N31 : THR_N));
  const rsrc_t rqA = mk(p.wqA, WBYTES), rqB = mk(p.wqB, WBYTES), roA = mk(p.woA, WBYTES), roB = mk(p.woB, WBYTES);
  const unsigned qvo = ((64 * wave + 16 * r) * HD + h * DH + 8 * c) * 2;
  const unsigned qvoB = ((64 * wave + 16 * r) * HD + hB * DH + 8 * c) * 2;
  const unsigned ovo = ((h * DH + (lane >> 3)) * DM + 64 * wave + 8 * (lane & 7)) * 2;
  const unsigned ovoB = ((hB * DH + (lane >> 3)) * DM + 64 * wave + 8 * (lane & 7)) * 2;

  u32x4 WA[NL], WB[NL], xv[2];
  {
    const char* xp = reinterpret_cast<const char*>(p.x) + (b * DM + 64 * wave + 16 * r) * 2;
    xv[0] = *reinterpret_cast<const u32x4*>(xp);
    xv[1] = *reinterpret_cast<const u32x4*>(xp + 16);
  }
  unsigned* cnt = reinterpret_cast<unsigned*>(lds) + L_CNT;
  const unsigned cnt_addr = (unsigned)(size_t)(__attribute__((address_space(3))) void*)cnt;
  if (tid == 0) *cnt = 0u;
  Str s{cx, rqA, rqB, qvo, qvoB};
  PIN();
  s.range<0, p_after(0)>();
  PIN();

  float* yb = p.y + (size_t)((b * NH + h) * 4 + half) * DM;
  float* mlb = p.ml + (size_t)((b * NH + h) * 4 + half) * 2;
  float* ybB = p.y + (size_t)((b * NH + hB) * 4 + 2 + half) * DM;
  float* mlbB = p.ml + (size_t)((b * NH + hB) * 4 + 2 + half) * 2;

  float qa[8];
#pragma unroll
  for (int k = 0; k < 8; ++k) qa[k] = 0.f;
  qb_steps<0, NL>(s, qa, xv);
  q_post(qa, lds + L_QP, wave, r, c);
  __syncthreads();

  u32x4 q = q_gather(lds + L_QP, c);
  State st;
  float x[NL], pr;
  reset(st);
#pragma unroll
  for (int k = 0; k < 8; ++k) qa[k] = 0.f;
#pragma unroll
  for (int i = 0; i < NL; ++i) x[i] = qk(s.S[i], q);
  PIN();
  qa_steps<0, 1>(s, qa, xv);
  {
    const float s0 = score(x, c);
    st.m = row_max(s0);
    pr = __builtin_amdgcn_exp2f(s0 - st.m);
    st.l = pr;
  }
  PIN();
  qa_steps<1, 2>(s, qa, xv);
  pv_steps<16, 0, 4>(s, st, pr);   PIN(); qa_steps<2, 3>(s, qa, xv);
  pv_steps<16, 4, 8>(s, st, pr);   PIN(); qa_steps<3, 4>(s, qa, xv);
  pv_steps<16, 8, 12>(s, st, pr);  PIN(); qa_steps<4, 5>(s, qa, xv);
  pv_steps<16, 12, 16>(s, st, pr); PIN(); qa_steps<5, 6>(s, qa, xv);
  wave_record(st, lds + L_REC + NW * REC, wave, r, c, lane);
  reset(st);
  PIN();
  qa_steps<6, NL>(s, qa, xv);
  q_post(qa, lds + L_QP + NW * 128, wave, r, c);
  lds_arrive(cnt_addr);
  PIN();
  s.paced<p_after(32), p_after(32) + DPOST>();
  lds_wait<64 * NW>(cnt_addr);
  q = q_gather(lds + L_QP + NW * 128, c);
  PIN();

  float sc[4];
  k_loop<2, 0>(s, q, x, WA, WB, roA, roB, ovo, ovoB);
  sc[0] = score(x, c);
  k_loop<3, 0>(s, q, x, WA, WB, roA, roB, ovo, ovoB);
  sc[1] = score(x, c);
  k_loop<4, 0>(s, q, x, WA, WB, roA, roB, ovo, ovoB);
  sc[2] = score(x, c);
  wg_merge(lds + L_REC + NW * REC, reinterpret_cast<unsigned*>(lds) + L_OB + wave * 64, lane, mlbB, wave == 0);
  k_loop<5, 0>(s, q, x, WA, WB, roA, roB, ovo, ovoB);
  sc[3] = score(x, c);
  st.m = row_max(fmaxf(fmaxf(sc[0], sc[1]), fmaxf(sc[2], sc[3])));
#pragma unroll
  for (int i = 0; i < 4; ++i) sc[i] = __builtin_amdgcn_exp2f(sc[i] - st.m);
  st.l = (sc[0] + sc[1]) + (sc[2] + sc[3]);
  v_loop<6, 0>(s, st, sc[0], WA, WB, roA, roB, ovo, ovoB);
  v_loop<7, 0>(s, st, sc[1], WA, WB, roA, roB, ovo, ovoB);
  wo_pair(WB);
  out_proj(WB, lds + L_OB + wave * 64, wave, lane, ybB);
  v_loop<8, 0>(s, st, sc[2], WA, WB, roA, roB, ovo, ovoB);
  v_loop<9, 0, 7>(s, st, sc[3], WA, WB, roA, roB, ovo, ovoB);
  wo_pair(WA);
#pragma unroll
  for (int i = 0; i < NL; ++i) asm volatile("" : "+v"(WA[i]));
  PIN();
  v_loop<9, 7>(s, st, sc[3], WA, WB, roA, roB, ovo, ovoB);

  wave_record(st, lds + L_REC, wave, r, c, lane);
  __syncthreads();
  if (wave == 0) wg_merge(lds + L_REC, reinterpret_cast<unsigned*>(lds) + L_O, lane, mlb, true);
  __syncthreads();
  out_proj(WA, lds + L_O, wave, lane, yb);
}

__global__ void __launch_bounds__(256) combine(Params p) {
  const int b = blockIdx.x >> 4;
  const int tid = threadIdx.x, lane = tid & 63;
  const int col0 = (blockIdx.x & 15) * 32 + (tid >> 6) * 8 + (lane >> 5) * 4;
  const int h = lane & 31;
  const float* ml = p.ml + (size_t)(b * NH + h) * 8;
  const f32x4 mlA = *reinterpret_cast<const f32x4*>(ml);
  const f32x4 mlB = *reinterpret_cast<const f32x4*>(ml + 4);
  const float* y = p.y + (size_t)(b * NH + h) * 4 * DM + col0;
  const f32x4 y0 = *reinterpret_cast<const f32x4*>(y);
  const f32x4 y1 = *reinterpret_cast<const f32x4*>(y + DM);
  const f32x4 y2 = *reinterpret_cast<const f32x4*>(y + 2 * DM);
  const f32x4 y3 = *reinterpret_cast<const f32x4*>(y + 3 * DM);
  const u32x4 xw = reinterpret_cast<const u32x4*>(p.x)[b * (DM / 8) + lane];
  const u32x4 gw = reinterpret_cast<const u32x4*>(p.gate)[lane];
  float gs = 0.f;
#pragma unroll
  for (int k = 0; k < 4; ++k) {
    const unsigned pr = cvtpk(bf_lo(xw[k]) * bf_lo(gw[k]), bf_hi(xw[k]) * bf_hi(gw[k]));
    gs += bf_lo(pr) + bf_hi(pr);
  }
  const float z = rbf(xrow_sum(row_sum(gs)));
  const float g = rbf(1.f / (1.f + __expf(-z)));
  const float omg = rbf(1.f - g);

  const float MA = fmaxf(mlA[0], mlA[2]), MB = fmaxf(mlB[0], mlB[2]);
  const float a0 = __builtin_amdgcn_exp2f(mlA[0] - MA), a1 = __builtin_amdgcn_exp2f(mlA[2] - MA);
  const float b0 = __builtin_amdgcn_exp2f(mlB[0] - MB), b1 = __builtin_amdgcn_exp2f(mlB[2] - MB);
  const float iA = 1.f / (a0 * mlA[1] + a1 * mlA[3]);
  const float iB = 1.f / (b0 * mlB[1] + b1 * mlB[3]);
  const f32x4 sa = (y0 * a0 + y1 * a1) * iA;
  const f32x4 sb = (y2 * b0 + y3 * b1) * iB;
  float u[4];
#pragma unroll
  for (int k = 0; k < 4; ++k) u[k] = tr16(sa[k], sb[k]);
  const int c = lane & 15;
  float v0, v1;
  { const float t1 = u[0] + dpp<XOR8>(u[0]), t2 = u[2] + dpp<XOR8>(u[2]); v0 = (c & 8) ? t2 : t1; }
  { const float t1 = u[1] + dpp<XOR8>(u[1]), t2 = u[3] + dpp<XOR8>(u[3]); v1 = (c & 8) ? t2 : t1; }
  float w;
  { const float t1 = v0 + dpp<XOR7>(v0), t2 = v1 + dpp<XOR7>(v1); w = (c & 4) ? t2 : t1; }
  w += dpp<XOR2>(w);
  w += dpp<XOR1>(w);
  const float t = rbf(rbf(w) * ((lane & 16) ? omg : g));
  const float o = tr16(t, t);
  if ((lane & 19) == 0) p.out[b * DM + col0 + (c >> 2)] = __float2bfloat16(o);
}

}

extern "C" void kernel_launch(void* const* d_in, const int* in_sizes, int n_in, void* d_out, int out_size, void* d_ws,
                              size_t ws_size, hipStream_t stream) {
  Params p;
  p.x = (const uint16_t*)d_in[0];
  p.kA = (const uint16_t*)d_in[1];
  p.vA = (const uint16_t*)d_in[2];
  p.wqA = (const uint16_t*)d_in[3];
  p.woA = (const uint16_t*)d_in[4];
  p.kB = (const uint16_t*)d_in[6];
  p.vB = (const uint16_t*)d_in[7];
  p.wqB = (const uint16_t*)d_in[8];
  p.woB = (const uint16_t*)d_in[9];
  p.gate = (const uint16_t*)d_in[10];
  p.y = (float*)d_ws;
  p.ml = (float*)((char*)d_ws + (size_t)NB * NH * 4 * DM * 4);
  p.out = (__hip_bfloat16*)d_out;
  attn_partial<<<256, NT, 0, stream>>>(p);
  combine<<<64, 256, 0, stream>>>(p);
}
```
